```python
import torch

def _softmax(x, axis=-1):
    return torch.softmax(x, dim=axis)
def _gelu(x):
    return torch.nn.functional.gelu(x, approximate="tanh")
def _silu(x):
    return torch.nn.functional.silu(x)
def _sigmoid(x):
    return torch.sigmoid(x)
def _layernorm(x, axis=-1, eps=1e-5):
    m = x.mean(dim=axis, keepdim=True)
    v = x.var(dim=axis, keepdim=True, unbiased=False)
    return (x - m) / torch.sqrt(v + eps)
def _scatter_add(tgt, idx, src, axis=0):
    return tgt.clone().index_add_(axis, idx, src)
def _segment_sum(src, seg, n):
    out = torch.zeros((n,) + src.shape[1:], dtype=src.dtype)
    return out.index_add_(0, seg, src)
def _pad(x, pw):
    flat = tuple(v for pair in reversed(pw) for v in pair)
    return torch.nn.functional.pad(x, flat)

kernel_name = 'parallel_sum__ssm_conv_s4d__glu_gated_bilinear__prompt'

def setup_inputs():

    _w = (lambda a: (lambda t: t.to(torch.bfloat16) if t.is_floating_point() else t)(torch.as_tensor(a)))
    def _A_setup():
        Bsz, L, D, N = 1, 2048, 512, 128
        return {
            "u": _w((lambda *s: torch.randn(*s, dtype=torch.bfloat16) * 0.05)(Bsz, L, D)),
            "log_A_real": _w((lambda *s: torch.randn(*s, dtype=torch.bfloat16) * 6.4)(D, N) * 0.5),
            "A_imag": _w((lambda *s: torch.randn(*s, dtype=torch.bfloat16) * 0.05)(D, N) * 3.14),
            "C_real": _w((lambda *s: torch.randn(*s, dtype=torch.bfloat16) * 0.05)(D, N) / N ** 0.5),
            "C_imag": _w((lambda *s: torch.randn(*s, dtype=torch.bfloat16) * 0.05)(D, N) / N ** 0.5),
            "log_dt": _w((lambda *s: torch.randn(*s, dtype=torch.bfloat16) * 0.05)(D) * 0.5 - 4.0),
            "D_skip": _w((lambda *s: torch.randn(*s, dtype=torch.bfloat16) * 0.05)(D)),
        }
    _A_in = _A_setup()

    def _B_setup():
        B, S, D, F = 1, 2048, 512, 1792
        return {
            "x": _w((lambda *s: torch.randn(*s, dtype=torch.bfloat16) * 0.05)(B, S, D)),
            "w_gate": _w((lambda *s: torch.randn(*s, dtype=torch.bfloat16) * 0.4)(D, F) * (D ** -0.5)),
            "w_up": _w((lambda *s: torch.randn(*s, dtype=torch.bfloat16) * 0.4)(D, F) * (D ** -0.5)),
            "w_down": _w((lambda *s: torch.randn(*s, dtype=torch.bfloat16) * 0.4)(F, D) * (F ** -0.5)),
        }
    _B_in = _B_setup()

    return {
        "A_u": _A_in["u"],
        "A_log_A_real": _A_in["log_A_real"],
        "A_A_imag": _A_in["A_imag"],
        "A_C_real": _A_in["C_real"],
        "A_C_imag": _A_in["C_imag"],
        "A_log_dt": _A_in["log_dt"],
        "A_D_skip": _A_in["D_skip"],
        "B_x": _B_in["x"],
        "B_w_gate": _B_in["w_gate"],
        "B_w_up": _B_in["w_up"],
        "B_w_down": _B_in["w_down"],
    }

def reference(A_u, A_log_A_real, A_A_imag, A_C_real, A_C_imag, A_log_dt, A_D_skip, B_x, B_w_gate, B_w_up, B_w_down):
    def _A_ref(u, log_A_real, A_imag, C_real, C_imag, log_dt, D_skip):
        L = u.shape[1]
        dt = torch.exp(log_dt)
        A = -torch.exp(log_A_real) + 1j * A_imag
        C = (C_real + 1j * C_imag) * dt[:, None]
        pos = (lambda x, dt: x.to(dt))(torch.arange(L), log_dt.dtype)
        P = torch.exp((A * dt[:, None])[:, :, None] * pos[None, None, :])
        K = 2.0 * torch.einsum("dn,dnl->dl", C, P).real
        L2 = 2 * L
        u_t = (lambda x, *d: x.permute(*d))(u, 0, 2, 1)
        Kf = (lambda x, *a, **k: torch.fft.rfft(x.float(), *a, **k))(K, L2)
        Uf = (lambda x, *a, **k: torch.fft.rfft(x.float(), *a, **k))(u_t, L2)
        y = (lambda x, *a, **k: torch.fft.irfft(x, *a, **k).to(torch.bfloat16))(Uf * Kf[None, :, :], L2)[:, :, :L]
        y = (lambda x, *d: x.permute(*d))(y, 0, 2, 1)
        return y + u * D_skip[None, None, :]

    def _B_ref(x, w_gate, w_up, w_down):
        g = torch.einsum("bsd,df->bsf", x, w_gate)
        u = torch.einsum("bsd,df->bsf", x, w_up)
        h = g * u
        return torch.einsum("bsf,fd->bsd", h, w_down)

    x = A_u
    xn = _layernorm(x, axis=-1)
    return x + _A_ref(xn, A_log_A_real, A_A_imag, A_C_real, A_C_imag, A_log_dt, A_D_skip) + _B_ref(xn, B_w_gate, B_w_up, B_w_down)
```

```cpp
#include <hip/hip_runtime.h>
#include <hip/hip_bf16.h>
#define POS_ROUND 0
typedef __hip_bfloat16 bf16;
__device__ __forceinline__ float bf(float x){ return __bfloat162float(__float2bfloat16(x)); }
constexpr int L=2048,D=512,N=128,F=1792;
__global__ void k_ln(const bf16* x, float* xn){
  int row=blockIdx.x; int t=threadIdx.x;
  float v[8]; float s=0;
  for(int i=0;i<8;i++){v[i]=__bfloat162float(x[row*D+t*8+i]); s+=v[i];}
  for(int o=32;o;o>>=1) s+=__shfl_xor(s,o);
  float m=s/D; float q=0;
  for(int i=0;i<8;i++){q+=(v[i]-m)*(v[i]-m);}
  for(int o=32;o;o>>=1) q+=__shfl_xor(q,o);
  float var=bf(q/D); float mb=bf(m);
  float sd=bf(sqrtf(bf(var+1e-5f)));
  for(int i=0;i<8;i++) xn[row*D+t*8+i]=bf(bf(v[i]-mb)/sd);
}
__global__ void k_kgen(const bf16* lar,const bf16* aim,const bf16* cr,const bf16* ci,const bf16* ldt,float* K){
  int d=blockIdx.y; int l=blockIdx.x*256+threadIdx.x;
  float dt=bf(expf(__bfloat162float(ldt[d])));
  float pos=POS_ROUND? bf((float)l):(float)l;
  float acc=0;
  for(int n=0;n<N;n++){
    float ar=-bf(expf(__bfloat162float(lar[d*N+n]))); float ai=__bfloat162float(aim[d*N+n]);
    float zr=ar*dt*pos, zi=ai*dt*pos;
    float e=expf(zr); float s,c; sincosf(zi,&s,&c);
    float Cr=__bfloat162float(cr[d*N+n])*dt, Ci=__bfloat162float(ci[d*N+n])*dt;
    acc+=Cr*e*c-Ci*e*s;
  }
  K[d*L+l]=2*acc;
}
__global__ void k_conv(const float* xn,const float* K,float* y){
  int d=blockIdx.y*64+(threadIdx.x&63); int l=blockIdx.x*4+(threadIdx.x>>6);
  float acc=0;
  for(int j=0;j<=l;j++) acc+=K[d*L+j]*xn[(l-j)*D+d];
  y[l*D+d]=acc;
}
__global__ void k_g1(const float* xn,const bf16* wg,const bf16* wu,float* h){
  int f=blockIdx.x*64+(threadIdx.x&63); int m=blockIdx.y*4+(threadIdx.x>>6);
  float g=0,u=0;
  for(int k=0;k<D;k++){float a=xn[m*D+k]; g+=a*__bfloat162float(wg[k*F+f]); u+=a*__bfloat162float(wu[k*F+f]);}
  h[m*F+f]=bf(bf(g)*bf(u));
}
__global__ void k_g2(const bf16* x,const float* xn,const float* y,const bf16* dsk,const float* h,const bf16* wd,bf16* out){
  int d=blockIdx.x*64+(threadIdx.x&63); int m=blockIdx.y*4+(threadIdx.x>>6);
  float a=0;
  for(int k=0;k<F;k++) a+=h[m*F+k]*__bfloat162float(wd[k*D+d]);
  float ya=bf(bf(y[m*D+d])+bf(xn[m*D+d]*__bfloat162float(dsk[d])));
  float r=bf(__bfloat162float(x[m*D+d])+ya)+bf(a);
  out[m*D+d]=__float2bfloat16(r);
}
extern "C" void kernel_launch(void* const* d_in, const int* in_sizes, int n_in,
                              void* d_out, int out_size, void* d_ws, size_t ws_size,
                              hipStream_t stream) {
  const bf16* x=(const bf16*)d_in[0];
  float* xn=(float*)d_ws; float* K=xn+L*D; float* y=K+L*D; float* h=y+L*D;
  k_ln<<<L,64,0,stream>>>(x,xn);
  k_kgen<<<dim3(L/256,D),256,0,stream>>>((const bf16*)d_in[1],(const bf16*)d_in[2],(const bf16*)d_in[3],(const bf16*)d_in[4],(const bf16*)d_in[5],K);
  k_conv<<<dim3(L/4,D/64),256,0,stream>>>(xn,K,y);
  k_g1<<<dim3(F/64,L/4),256,0,stream>>>(xn,(const bf16*)d_in[8],(const bf16*)d_in[9],h);
  k_g2<<<dim3(D/64,L/4),256,0,stream>>>(x,xn,y,(const bf16*)d_in[6],h,(const bf16*)d_in[10],(bf16*)d_out);
}
```

```cpp
#include <hip/hip_runtime.h>
#include <hip/hip_bf16.h>
#define POS_ROUND 1
typedef __hip_bfloat16 bf16;
__device__ __forceinline__ float bf(float x){ return __bfloat162float(__float2bfloat16(x)); }
constexpr int L=2048,D=512,N=128,F=1792;
__global__ void k_ln(const bf16* x, float* xn){
  int row=blockIdx.x; int t=threadIdx.x;
  float v[8]; float s=0;
  for(int i=0;i<8;i++){v[i]=__bfloat162float(x[row*D+t*8+i]); s+=v[i];}
  for(int o=32;o;o>>=1) s+=__shfl_xor(s,o);
  float m=s/D; float q=0;
  for(int i=0;i<8;i++){q+=(v[i]-m)*(v[i]-m);}
  for(int o=32;o;o>>=1) q+=__shfl_xor(q,o);
  float var=bf(q/D); float mb=bf(m);
  float sd=bf(sqrtf(bf(var+1e-5f)));
  for(int i=0;i<8;i++) xn[row*D+t*8+i]=bf(bf(v[i]-mb)/sd);
}
__global__ void k_kgen(const bf16* lar,const bf16* aim,const bf16* cr,const bf16* ci,const bf16* ldt,float* K){
  int d=blockIdx.y; int l=blockIdx.x*256+threadIdx.x;
  float dt=bf(expf(__bfloat162float(ldt[d])));
  float pos=POS_ROUND? bf((float)l):(float)l;
  float acc=0;
  for(int n=0;n<N;n++){
    float ar=-bf(expf(__bfloat162float(lar[d*N+n]))); float ai=__bfloat162float(aim[d*N+n]);
    float zr=ar*dt*pos, zi=ai*dt*pos;
    float e=expf(zr); float s,c; sincosf(zi,&s,&c);
    float Cr=__bfloat162float(cr[d*N+n])*dt, Ci=__bfloat162float(ci[d*N+n])*dt;
    acc+=Cr*e*c-Ci*e*s;
  }
  K[d*L+l]=2*acc;
}
__global__ void k_conv(const float* xn,const float* K,float* y){
  int d=blockIdx.y*64+(threadIdx.x&63); int l=blockIdx.x*4+(threadIdx.x>>6);
  float acc=0;
  for(int j=0;j<=l;j++) acc+=K[d*L+j]*xn[(l-j)*D+d];
  y[l*D+d]=acc;
}
__global__ void k_g1(const float* xn,const bf16* wg,const bf16* wu,float* h){
  int f=blockIdx.x*64+(threadIdx.x&63); int m=blockIdx.y*4+(threadIdx.x>>6);
  float g=0,u=0;
  for(int k=0;k<D;k++){float a=xn[m*D+k]; g+=a*__bfloat162float(wg[k*F+f]); u+=a*__bfloat162float(wu[k*F+f]);}
  h[m*F+f]=bf(bf(g)*bf(u));
}
__global__ void k_g2(const bf16* x,const float* xn,const float* y,const bf16* dsk,const float* h,const bf16* wd,bf16* out){
  int d=blockIdx.x*64+(threadIdx.x&63); int m=blockIdx.y*4+(threadIdx.x>>6);
  float a=0;
  for(int k=0;k<F;k++) a+=h[m*F+k]*__bfloat162float(wd[k*D+d]);
  float ya=bf(bf(y[m*D+d])+bf(xn[m*D+d]*__bfloat162float(dsk[d])));
  float r=bf(__bfloat162float(x[m*D+d])+ya)+bf(a);
  out[m*D+d]=__float2bfloat16(r);
}
#define PMODE 0

typedef unsigned long long u64;
#define GLDS(g,l) __builtin_amdgcn_global_load_lds((const __attribute__((address_space(1))) void*)(g),(__attribute__((address_space(3))) void*)(l),16,0,0)
__global__ __launch_bounds__(256,1) void probe(const char* wd,const char* x,unsigned* sink){
  __shared__ __attribute__((aligned(16))) char sm[65536];
  u64 c0=__builtin_amdgcn_s_memtime();
  int b=blockIdx.x; int idx=b>>3; int nj=idx>>2;
  int t=threadIdx.x, w=t>>6, lane=t&63;
  unsigned acc=0;
  __syncthreads();
  acc^=*(unsigned*)(sm+t*4);
  if(acc==0x12345678) sink[t]=acc;
  u64 c1=__builtin_amdgcn_s_memtime();
  if(b==100 && t==0){
    u64 r0=__builtin_amdgcn_s_memrealtime();
    u64 dur=(c1-c0)*40;
    if(dur>2000000) dur=2000000;
    while(__builtin_amdgcn_s_memrealtime()-r0<dur) __builtin_amdgcn_s_sleep(10);
  }
}
extern "C" void kernel_launch(void* const* d_in, const int* in_sizes, int n_in,
                              void* d_out, int out_size, void* d_ws, size_t ws_size,
                              hipStream_t stream) {
  const bf16* x=(const bf16*)d_in[0];
  float* xn=(float*)d_ws; float* K=xn+L*D; float* y=K+L*D; float* h=y+L*D;
  probe<<<256,256,0,stream>>>((const char*)d_in[10],(const char*)d_in[0],(unsigned*)d_ws);
  k_ln<<<L,64,0,stream>>>(x,xn);
  k_kgen<<<dim3(L/256,D),256,0,stream>>>((const bf16*)d_in[1],(const bf16*)d_in[2],(const bf16*)d_in[3],(const bf16*)d_in[4],(const bf16*)d_in[5],K);
  k_conv<<<dim3(L/4,D/64),256,0,stream>>>(xn,K,y);
  k_g1<<<dim3(F/64,L/4),256,0,stream>>>(xn,(const bf16*)d_in[8],(const bf16*)d_in[9],h);
  k_g2<<<dim3(D/64,L/4),256,0,stream>>>(x,xn,y,(const bf16*)d_in[6],h,(const bf16*)d_in[10],(bf16*)d_out);
}
```

```cpp
#include <hip/hip_runtime.h>
#include <hip/hip_bf16.h>
#define POS_ROUND 1
typedef __hip_bfloat16 bf16;
__device__ __forceinline__ float bf(float x){ return __bfloat162float(__float2bfloat16(x)); }
constexpr int L=2048,D=512,N=128,F=1792;
__global__ void k_ln(const bf16* x, float* xn){
  int row=blockIdx.x; int t=threadIdx.x;
  float v[8]; float s=0;
  for(int i=0;i<8;i++){v[i]=__bfloat162float(x[row*D+t*8+i]); s+=v[i];}
  for(int o=32;o;o>>=1) s+=__shfl_xor(s,o);
  float m=s/D; float q=0;
  for(int i=0;i<8;i++){q+=(v[i]-m)*(v[i]-m);}
  for(int o=32;o;o>>=1) q+=__shfl_xor(q,o);
  float var=bf(q/D); float mb=bf(m);
  float sd=bf(sqrtf(bf(var+1e-5f)));
  for(int i=0;i<8;i++) xn[row*D+t*8+i]=bf(bf(v[i]-mb)/sd);
}
__global__ void k_kgen(const bf16* lar,const bf16* aim,const bf16* cr,const bf16* ci,const bf16* ldt,float* K){
  int d=blockIdx.y; int l=blockIdx.x*256+threadIdx.x;
  float dt=bf(expf(__bfloat162float(ldt[d])));
  float pos=POS_ROUND? bf((float)l):(float)l;
  float acc=0;
  for(int n=0;n<N;n++){
    float ar=-bf(expf(__bfloat162float(lar[d*N+n]))); float ai=__bfloat162float(aim[d*N+n]);
    float zr=ar*dt*pos, zi=ai*dt*pos;
    float e=expf(zr); float s,c; sincosf(zi,&s,&c);
    float Cr=__bfloat162float(cr[d*N+n])*dt, Ci=__bfloat162float(ci[d*N+n])*dt;
    acc+=Cr*e*c-Ci*e*s;
  }
  K[d*L+l]=2*acc;
}
__global__ void k_conv(const float* xn,const float* K,float* y){
  int d=blockIdx.y*64+(threadIdx.x&63); int l=blockIdx.x*4+(threadIdx.x>>6);
  float acc=0;
  for(int j=0;j<=l;j++) acc+=K[d*L+j]*xn[(l-j)*D+d];
  y[l*D+d]=acc;
}
__global__ void k_g1(const float* xn,const bf16* wg,const bf16* wu,float* h){
  int f=blockIdx.x*64+(threadIdx.x&63); int m=blockIdx.y*4+(threadIdx.x>>6);
  float g=0,u=0;
  for(int k=0;k<D;k++){float a=xn[m*D+k]; g+=a*__bfloat162float(wg[k*F+f]); u+=a*__bfloat162float(wu[k*F+f]);}
  h[m*F+f]=bf(bf(g)*bf(u));
}
__global__ void k_g2(const bf16* x,const float* xn,const float* y,const bf16* dsk,const float* h,const bf16* wd,bf16* out){
  int d=blockIdx.x*64+(threadIdx.x&63); int m=blockIdx.y*4+(threadIdx.x>>6);
  float a=0;
  for(int k=0;k<F;k++) a+=h[m*F+k]*__bfloat162float(wd[k*D+d]);
  float ya=bf(bf(y[m*D+d])+bf(xn[m*D+d]*__bfloat162float(dsk[d])));
  float r=bf(__bfloat162float(x[m*D+d])+ya)+bf(a);
  out[m*D+d]=__float2bfloat16(r);
}
#define PMODE 0

typedef unsigned long long u64;
#define GLDS(g,l) __builtin_amdgcn_global_load_lds((const __attribute__((address_space(1))) void*)(g),(__attribute__((address_space(3))) void*)(l),16,0,0)
__global__ __launch_bounds__(512,1) void probe(const char* wd,const char* x,const char* wg,const char* wu,unsigned* sink){
  __shared__ __attribute__((aligned(16))) char sm[131072];
  u64 c0=__builtin_amdgcn_s_memtime();
  int b=blockIdx.x; int xcd=b&7; int idx=b>>3; int nj=idx>>2;
  int t=threadIdx.x, w=t>>6, lane=t&63;
  unsigned acc=0;
  *(unsigned*)(sm+t*4)=t;
  __syncthreads();
  acc^=*(unsigned*)(sm+t*4);
  if(acc==0x12345678) sink[t]=acc;
  u64 c1=__builtin_amdgcn_s_memtime();
  if(b==100 && t==0){
    u64 r0=__builtin_amdgcn_s_memrealtime();
    u64 dur=(c1-c0)*40;
    if(dur>2000000) dur=2000000;
    while(__builtin_amdgcn_s_memrealtime()-r0<dur) __builtin_amdgcn_s_sleep(10);
  }
}
extern "C" void kernel_launch(void* const* d_in, const int* in_sizes, int n_in,
                              void* d_out, int out_size, void* d_ws, size_t ws_size,
                              hipStream_t stream) {
  const bf16* x=(const bf16*)d_in[0];
  float* xn=(float*)d_ws; float* K=xn+L*D; float* y=K+L*D; float* h=y+L*D;
  probe<<<256,512,0,stream>>>((const char*)d_in[10],(const char*)d_in[0],(const char*)d_in[8],(const char*)d_in[9],(unsigned*)d_ws);
  k_ln<<<L,64,0,stream>>>(x,xn);
  k_kgen<<<dim3(L/256,D),256,0,stream>>>((const bf16*)d_in[1],(const bf16*)d_in[2],(const bf16*)d_in[3],(const bf16*)d_in[4],(const bf16*)d_in[5],K);
  k_conv<<<dim3(L/4,D/64),256,0,stream>>>(xn,K,y);
  k_g1<<<dim3(F/64,L/4),256,0,stream>>>(xn,(const bf16*)d_in[8],(const bf16*)d_in[9],h);
  k_g2<<<dim3(D/64,L/4),256,0,stream>>>(x,xn,y,(const bf16*)d_in[6],h,(const bf16*)d_in[10],(bf16*)d_out);
}
```

```cpp
#include <hip/hip_runtime.h>
#include <hip/hip_bf16.h>
#define POS_ROUND 1
typedef __hip_bfloat16 bf16;
__device__ __forceinline__ float bf(float x){ return __bfloat162float(__float2bfloat16(x)); }
constexpr int L=2048,D=512,N=128,F=1792;
__global__ void k_ln(const bf16* x, float* xn){
  int row=blockIdx.x; int t=threadIdx.x;
  float v[8]; float s=0;
  for(int i=0;i<8;i++){v[i]=__bfloat162float(x[row*D+t*8+i]); s+=v[i];}
  for(int o=32;o;o>>=1) s+=__shfl_xor(s,o);
  float m=s/D; float q=0;
  for(int i=0;i<8;i++){q+=(v[i]-m)*(v[i]-m);}
  for(int o=32;o;o>>=1) q+=__shfl_xor(q,o);
  float var=bf(q/D); float mb=bf(m);
  float sd=bf(sqrtf(bf(var+1e-5f)));
  for(int i=0;i<8;i++) xn[row*D+t*8+i]=bf(bf(v[i]-mb)/sd);
}
__global__ void k_kgen(const bf16* lar,const bf16* aim,const bf16* cr,const bf16* ci,const bf16* ldt,float* K){
  int d=blockIdx.y; int l=blockIdx.x*256+threadIdx.x;
  float dt=bf(expf(__bfloat162float(ldt[d])));
  float pos=POS_ROUND? bf((float)l):(float)l;
  float acc=0;
  for(int n=0;n<N;n++){
    float ar=-bf(expf(__bfloat162float(lar[d*N+n]))); float ai=__bfloat162float(aim[d*N+n]);
    float zr=ar*dt*pos, zi=ai*dt*pos;
    float e=expf(zr); float s,c; sincosf(zi,&s,&c);
    float Cr=__bfloat162float(cr[d*N+n])*dt, Ci=__bfloat162float(ci[d*N+n])*dt;
    acc+=Cr*e*c-Ci*e*s;
  }
  K[d*L+l]=2*acc;
}
__global__ void k_conv(const float* xn,const float* K,float* y){
  int d=blockIdx.y*64+(threadIdx.x&63); int l=blockIdx.x*4+(threadIdx.x>>6);
  float acc=0;
  for(int j=0;j<=l;j++) acc+=K[d*L+j]*xn[(l-j)*D+d];
  y[l*D+d]=acc;
}
__global__ void k_g1(const float* xn,const bf16* wg,const bf16* wu,float* h){
  int f=blockIdx.x*64+(threadIdx.x&63); int m=blockIdx.y*4+(threadIdx.x>>6);
  float g=0,u=0;
  for(int k=0;k<D;k++){float a=xn[m*D+k]; g+=a*__bfloat162float(wg[k*F+f]); u+=a*__bfloat162float(wu[k*F+f]);}
  h[m*F+f]=bf(bf(g)*bf(u));
}
__global__ void k_g2(const bf16* x,const float* xn,const float* y,const bf16* dsk,const float* h,const bf16* wd,bf16* out){
  int d=blockIdx.x*64+(threadIdx.x&63); int m=blockIdx.y*4+(threadIdx.x>>6);
  float a=0;
  for(int k=0;k<F;k++) a+=h[m*F+k]*__bfloat162float(wd[k*D+d]);
  float ya=bf(bf(y[m*D+d])+bf(xn[m*D+d]*__bfloat162float(dsk[d])));
  float r=bf(__bfloat162float(x[m*D+d])+ya)+bf(a);
  out[m*D+d]=__float2bfloat16(r);
}
#define PRE 1
#define MAIN 0
#define AUX 0

typedef unsigned long long u64;
#define GLDS(g,l) __builtin_amdgcn_global_load_lds((const __attribute__((address_space(1))) void*)(g),(__attribute__((address_space(3))) void*)(l),16,0,AUX)
__global__ __launch_bounds__(512,1) void probe(const char* wd,const char* x,const char* wg,const char* wu,unsigned* sink){
  __shared__ __attribute__((aligned(16))) char sm[163840];
  u64 c0=__builtin_amdgcn_s_memtime();
  int b=blockIdx.x; int xcd=b&7; int idx=b>>3;
  int t=threadIdx.x, w=t>>6, lane=t&63;
  unsigned acc=0;
  *(unsigned*)(sm+t*4)=t;
  int mi=(xcd&3)*4+(idx&3), fj=(xcd>>2)*7+(idx>>2);
  if(PRE==1){
    GLDS(x+b*8192+w*1024+lane*16, sm+147456+w*1024);
    if(w<7){ GLDS(wg+b*7168+w*1024+lane*16, sm+147456+w*1024); GLDS(wu+b*7168+w*1024+lane*16, sm+147456+w*1024);}
    asm volatile("s_waitcnt vmcnt(0)":::"memory");
  }
  if(PRE==2){
    const char* xp=x+(size_t)((xcd&3)*512+idx*16+w*2)*1024+lane*16;
    GLDS(xp,sm+w*1024); GLDS(xp+1024,sm+8192+w*1024);
    #pragma unroll
    for(int i=0;i<7;i++){
      int c=(w>>1)*7*64+i*64+lane;
      int row=c/112, cc=c%112;
      GLDS(((w&1)?wu:wg)+(size_t)(idx*16+row)*3584+(xcd>>2)*1792+cc*16, sm+16384+(w*7+i)*1024);
    }
    asm volatile("s_waitcnt vmcnt(0)":::"memory");
  }
  if(MAIN && idx<28){
    const char* xp=x+(size_t)(mi*128+w*16+(lane>>3))*1024+(lane&7)*16;
    const char* wp=((w&1)?wu:wg)+(size_t)((w>>1)*16+(lane>>4))*3584+fj*256+(lane&15)*16;
    int s0=(MAIN==2)? ((idx&3)*2+(idx>>2)+ (xcd>>2)*4 + (xcd&3)*2) : (MAIN==3)? ((idx&1)*4+((idx>>2)&1)*4) : 0;
    #pragma unroll 1
    for(int ss=0;ss<8;ss++){
      int s=(ss+s0)&7;
      char* d=sm+(ss%3)*49152+w*6144;
      GLDS(xp+s*128, d); GLDS(xp+s*128+8192,d+1024);
      #pragma unroll
      for(int i=0;i<4;i++) GLDS(wp+(size_t)(s*64+i*4)*3584,d+2048+i*1024);
      asm volatile("s_waitcnt vmcnt(12)":::"memory");
    }
    asm volatile("s_waitcnt vmcnt(0)":::"memory");
  }
  __syncthreads();
  acc^=*(unsigned*)(sm+t*4);
  if(acc==0x12345678) sink[t]=acc;
  u64 c1=__builtin_amdgcn_s_memtime();
  if(b==100 && t==0){
    u64 r0=__builtin_amdgcn_s_memrealtime();
    u64 dur=(c1-c0)*40;
    if(dur>2000000) dur=2000000;
    while(__builtin_amdgcn_s_memrealtime()-r0<dur) __builtin_amdgcn_s_sleep(10);
  }
}
extern "C" void kernel_launch(void* const* d_in, const int* in_sizes, int n_in,
                              void* d_out, int out_size, void* d_ws, size_t ws_size,
                              hipStream_t stream) {
  const bf16* x=(const bf16*)d_in[0];
  float* xn=(float*)d_ws; float* K=xn+L*D; float* y=K+L*D; float* h=y+L*D;
  probe<<<256,512,0,stream>>>((const char*)d_in[10],(const char*)d_in[0],(const char*)d_in[8],(const char*)d_in[9],(unsigned*)d_ws);
  k_ln<<<L,64,0,stream>>>(x,xn);
  k_kgen<<<dim3(L/256,D),256,0,stream>>>((const bf16*)d_in[1],(const bf16*)d_in[2],(const bf16*)d_in[3],(const bf16*)d_in[4],(const bf16*)d_in[5],K);
  k_conv<<<dim3(L/4,D/64),256,0,stream>>>(xn,K,y);
  k_g1<<<dim3(F/64,L/4),256,0,stream>>>(xn,(const bf16*)d_in[8],(const bf16*)d_in[9],h);
  k_g2<<<dim3(D/64,L/4),256,0,stream>>>(x,xn,y,(const bf16*)d_in[6],h,(const bf16*)d_in[10],(bf16*)d_out);
}
```

```cpp
#include <hip/hip_runtime.h>
#include <hip/hip_bf16.h>

typedef unsigned u32;
typedef unsigned short u16;
typedef unsigned long long u64;
typedef u32 u32x2 __attribute__((ext_vector_type(2)));
typedef u32 u32x4 __attribute__((ext_vector_type(4)));
typedef float f32x2 __attribute__((ext_vector_type(2)));
typedef float f32x4 __attribute__((ext_vector_type(4)));
typedef float f32x16 __attribute__((ext_vector_type(16)));
typedef short s16x4 __attribute__((ext_vector_type(4)));
typedef __bf16 bf16x2 __attribute__((ext_vector_type(2)));
typedef __bf16 bf16x8 __attribute__((ext_vector_type(8)));
typedef _Float16 f16x2 __attribute__((ext_vector_type(2)));
typedef _Float16 f16x8 __attribute__((ext_vector_type(8)));
#define LDS __attribute__((address_space(3)))
#define GLB __attribute__((address_space(1)))
#define DEV __device__ __forceinline__

constexpr u32 WS_GEN = 0;
constexpr u32 WS_FS = 4096;
constexpr u32 WS_FH = WS_FS + 32 * 128;
constexpr u32 WS_FY = WS_FH + 2048 * 128;
constexpr u32 WS_MEAN = WS_FY + 1024 * 128;
constexpr u32 WS_RSTD = WS_MEAN + 8192;
constexpr u32 WS_STAMP = WS_RSTD + 8192;
constexpr u32 WS_U = 1u << 20;
constexpr u32 WS_Y = 3u << 20;
constexpr u32 WS_H = 5u << 20;
constexpr u32 WS_END = WS_H + 2048 * 1792 * 2;

constexpr u32 SLOT = 49152;
constexpr u32 L_SP = 3 * SLOT;
constexpr u32 L_KX = L_SP;
constexpr u32 L_POLL = L_SP;
constexpr u32 L_CS = L_SP + 1024;
constexpr u32 L_MU = L_CS + 512;
constexpr u32 L_CAR = L_MU + 1024;
constexpr u32 L_MISC = L_SP + 5120;
constexpr u32 L_DUMMY = L_MISC + 256;
constexpr u32 L_KA = L_SP + 5632;
constexpr u32 L_U = 2 * SLOT + 32768;
constexpr u32 L_PA01 = SLOT + 32768;
constexpr u32 L_PA23 = 2 * SLOT + 32768;
static_assert(L_DUMMY + 256 == L_KA && L_KA + 10752 == 163840 && L_CAR + 1056 <= L_MISC, "lds map");

constexpr u64 TAG_STEP = 0x9E3779B97F4A7C15ull;
constexpr int CONV_EARLY = 8, CONV_MID = 44;
constexpr u32 CODE_BYTES = 35840;
#define VMCNT(n) asm volatile("s_waitcnt vmcnt(%0)" ::"n"(n) : "memory")
#define LGKM0() asm volatile("s_waitcnt lgkmcnt(0)" ::: "memory")
#define BAR() __builtin_amdgcn_s_barrier()
#define SCHED() __builtin_amdgcn_sched_barrier(0)

#define STAMP(i)
#define STAMP_OUT()

DEV void dma16x4(const void* sb, u32 lds, u32 v0, u32 v1, u32 v2, u32 v3) {
  asm volatile("s_mov_b32 m0, %1\n\ts_nop 0\n\t"
               "global_load_lds_dwordx4 %2, %0\n\t"
               "global_load_lds_dwordx4 %3, %0 offset:1024\n\t"
               "global_load_lds_dwordx4 %4, %0 offset:2048\n\t"
               "global_load_lds_dwordx4 %5, %0 offset:3072" ::"s"(sb), "s"(lds), "v"(v0), "v"(v1), "v"(v2), "v"(v3) : "memory");
}
DEV void dma16x2_sc1(const void* sb, u32 lds, u32 v0) {
  asm volatile("s_mov_b32 m0, %1\n\ts_nop 0\n\t"
               "global_load_lds_dwordx4 %2, %0 sc1\n\t"
               "global_load_lds_dwordx4 %2, %0 offset:1024 sc1" ::"s"(sb), "s"(lds), "v"(v0) : "memory");
}
DEV void dma4(const void* sb, u32 lds, u32 v0) {
  asm volatile("s_mov_b32 m0, %1\n\ts_nop 0\n\tglobal_load_lds_dword %2, %0" ::"s"(sb), "s"(lds), "v"(v0) : "memory");
}
DEV void dma4_sc1(const void* sb, u32 lds, u32 v0) {
  asm volatile("s_mov_b32 m0, %1\n\ts_nop 0\n\tglobal_load_lds_dword %2, %0 sc1" ::"s"(sb), "s"(lds), "v"(v0) : "memory");
}
DEV void st16_sc1(const void* sb, u32 voff, u32x4 v) {
  asm volatile("global_store_dwordx4 %0, %1, %2 sc1\n\ts_nop 1" ::"v"(voff), "v"(v), "s"(sb) : "memory");
}
DEV float bflo(u32 v) { return __builtin_bit_cast(float, v << 16); }
DEV float bfhi(u32 v) { return __builtin_bit_cast(float, v & 0xffff0000u); }
DEV u32 pkbf(float a, float b) { f32x2 v = {a, b}; return __builtin_bit_cast(u32, __builtin_convertvector(v, bf16x2)); }
DEV u32 pkh(float a, float b) { f32x2 v = {a, b}; return __builtin_bit_cast(u32, __builtin_convertvector(v, f16x2)); }
DEV float rbf(float a) { return bflo(pkbf(a, a)); }
template <int CTRL> DEV float dppf(float v) { return __builtin_bit_cast(float, __builtin_amdgcn_update_dpp(0, __builtin_bit_cast(int, v), CTRL, 0xf, 0xf, true)); }
template <int CTRL> DEV u32 dppu(u32 v) { return (u32)__builtin_amdgcn_update_dpp(0, (int)v, CTRL, 0xf, 0xf, true); }
DEV f32x2 cmul(f32x2 a, f32x2 b) {
  f32x2 t = f32x2{a.x, a.x} * b;
  return __builtin_elementwise_fma(f32x2{a.y, a.y}, f32x2{-b.y, b.x}, t);
}
DEV u32x4 trpair(LDS char* p0, LDS char* p1) {
  u32x2 a = __builtin_bit_cast(u32x2, __builtin_amdgcn_ds_read_tr16_b64_v4i16((LDS s16x4*)p0));
  u32x2 c = __builtin_bit_cast(u32x2, __builtin_amdgcn_ds_read_tr16_b64_v4i16((LDS s16x4*)p1));
  return u32x4{a.x, a.y, c.x, c.y};
}
DEV f32x16 mma_bf(u32x4 a, u32x4 b, f32x16 c) {
  return __builtin_amdgcn_mfma_f32_32x32x16_bf16(__builtin_bit_cast(bf16x8, a), __builtin_bit_cast(bf16x8, b), c, 0, 0, 0);
}
DEV f32x16 mma_h(u32x4 a, u32x4 b, f32x16 c) {
  return __builtin_amdgcn_mfma_f32_32x32x16_f16(__builtin_bit_cast(f16x8, a), __builtin_bit_cast(f16x8, b), c, 0, 0, 0);
}
DEV void set_flag(char* ws, u32 off, u64 tag) {
  __hip_atomic_store((GLB u64*)(ws + off), tag, __ATOMIC_RELAXED, __HIP_MEMORY_SCOPE_AGENT);
}
DEV void wait_flags(char* ws, u32 off, bool act, u64 tag) {
#pragma unroll 1
  for (int spins = 0; spins < (1 << 18); ++spins) {
    u64 v = tag;
    if (act) v = __hip_atomic_load((GLB u64*)(ws + off), __ATOMIC_RELAXED, __HIP_MEMORY_SCOPE_AGENT);
    if (__all(v == tag)) break;
    __builtin_amdgcn_s_sleep(2);
  }
  asm volatile("" ::: "memory");
}
DEV void wait_lds_cnt(LDS char* sm, u32 off, u32 want) {
#pragma unroll 1
  for (int spins = 0; spins < (1 << 20); ++spins) {
    u32 v = __hip_atomic_load((LDS u32*)(sm + off), __ATOMIC_RELAXED, __HIP_MEMORY_SCOPE_WORKGROUP);
    if (__builtin_amdgcn_readfirstlane(v) >= want) break;
  }
  asm volatile("" ::: "memory");
}
DEV void lds_inc(LDS char* sm, u32 off, int lane) {
  if (lane == 0) asm volatile("ds_add_u32 %0, %1" ::"v"((u32)(size_t)sm + off), "v"(1u) : "memory");
}

template <int PS> struct ConvPlan {
  int k[72], t[72], first[72];
  constexpr ConvPlan() : k{}, t{}, first{} {
    const int T[4] = {PS ? 1 : 0, PS ? 2 : 3, PS ? 5 : 4, PS ? 6 : 7};
    int n = 0;
    for (int kk = 0; kk < 32; ++kk) {
      bool f = true;
      for (int i = 0; i < 4; ++i)
        if (kk < 4 * T[i] + 4) { k[n] = kk; t[n] = i; first[n] = f; f = false; ++n; }
    }
  }
};
template <int PS, class Early, class Mid>
DEV void conv_mm(LDS char* kp, LDS char* up, f32x16 (&acc)[4], Early early, Mid mid) {
  constexpr int T[4] = {PS ? 1 : 0, PS ? 2 : 3, PS ? 5 : 4, PS ? 6 : 7};
  constexpr ConvPlan<PS> P;
  constexpr int D = 4;
  u32x4 kf[32], uf[72];
  f32x16 zero;
#pragma unroll
  for (int e = 0; e < 16; ++e) zero[e] = 0.f;
  auto fetch = [&](int n) {
    if (P.first[n]) kf[P.k[n]] = trpair(kp + 128 * P.k[n], kp + 128 * P.k[n] - 32);
    uf[n] = *(LDS u32x4*)(up + 512 * T[P.t[n]] - 128 * P.k[n]);
  };
#pragma unroll
  for (int n = 0; n < D; ++n) fetch(n);
  SCHED();
#pragma unroll
  for (int n = 0; n < 72; ++n) {
    acc[P.t[n]] = mma_h(uf[n], kf[P.k[n]], P.k[n] == 0 ? zero : acc[P.t[n]]);
    SCHED();
    if (n + D < 72) fetch(n + D);
    if (n == CONV_EARLY) early();
    if (n == CONV_MID) mid();
    SCHED();
  }
}

__global__ __launch_bounds__(512, 2) void fused_kernel(const u16* __restrict__ x, const u16* __restrict__ lar, const u16* __restrict__ aim,
                                                       const u16* __restrict__ cre, const u16* __restrict__ cim, const u16* __restrict__ ldt,
                                                       const u16* __restrict__ dsk, const u16* __restrict__ wg, const u16* __restrict__ wu,
                                                       const u16* __restrict__ wd, u16* __restrict__ out, char* ws) {
  __shared__ __attribute__((aligned(16))) char smem[163840];
  LDS char* sm = (LDS char*)smem;
  const u32 sb = (u32)(size_t)sm;
  {
    u64 pc0; asm volatile("s_getpc_b64 %0" : "=s"(pc0));
    dma4((const char*)(pc0 & ~127ull), sb + L_DUMMY, min(((blockIdx.x >> 3) << 10) + ((threadIdx.x >> 6) << 7) + ((threadIdx.x & 1) << 15), CODE_BYTES - 128u));
  }
  asm volatile("" ::"s"(x), "s"(lar), "s"(aim), "s"(cre), "s"(cim), "s"(ldt), "s"(dsk), "s"(wg), "s"(wu), "s"(wd), "s"(out), "s"(ws));
  const int b = blockIdx.x, xcd = b & 7, idx = b >> 3;
  const int lane = threadIdx.x & 63;
  const int w = __builtin_amdgcn_readfirstlane(threadIdx.x >> 6), q = w & 3;
  const int a2 = xcd & 1, bq = xcd >> 1;
  const int mt = a2 * 4 + (idx & 3), ft = bq * 7 + (idx >> 2);
  const int s0 = (2 * bq + 4 * a2) & 7;
  const bool helper = idx >= 28;
  const int hb = a2 * 16 + bq * 4 + (idx & 3);
  const int rt = bq * 8 + (idx & 7), ct = a2 * 4 + (idx >> 3);
  const int B128 = rt >> 1, sh = rt & 1;
  const int h = lane >> 5;
  const int chl = q >> 1, ch = 2 * b + chl;
  const auto wsr = __builtin_amdgcn_make_buffer_rsrc(ws, 0, WS_END, 0x00020000);

  u32 l_lar, l_aim, l_cr, l_ci, l_ldt;
  u64 l_gen;
  asm volatile("global_load_ushort %0, %6, %8\n\tglobal_load_ushort %1, %6, %9\n\tglobal_load_ushort %2, %6, %10\n\t"
               "global_load_ushort %3, %6, %11\n\tglobal_load_ushort %4, %7, %12\n\tglobal_load_dwordx2 %5, %7, %13 sc1"
               : "=&v"(l_lar), "=&v"(l_aim), "=&v"(l_cr), "=&v"(l_ci), "=&v"(l_ldt), "=&v"(l_gen)
               : "v"((b * 256 + (threadIdx.x & 255)) * 2), "v"(0u), "s"(lar), "s"(aim), "s"(cre), "s"(cim), "s"(ldt + ch), "s"(ws + WS_GEN) : "memory");
  const char* xb = (const char*)x + (size_t)mt * 256 * 1024;
  const char* xtouch = xb + (idx >> 3) * 65536;
  if (w < 3) {
    const char* tb = q == 0 ? xtouch + (s0 + ((idx >> 2) & 1)) * 128
                            : (const char*)(q == 1 ? wg : wu) + (helper ? 0 : (((s0 + (idx & 3)) & 7) * 64) * 3584 + ft * 128);
    dma4(tb, sb + L_DUMMY, lane * (q == 0 ? 1024 : helper ? 0 : 3584));
  }
  const u32 cp = lane & 7, l3 = lane >> 3, l4 = lane >> 4;
  u32 vx[4], vw[4];
#pragma unroll
  for (int j = 0; j < 4; ++j) {
    vx[j] = (64 * q + 8 * j + l3) * 1024 + ((cp ^ ((4 * j + l4) & 7)) << 4) - 1024 * j;
    vw[j] = (32 * (q & 1) + 8 * j + l3) * 3584 + ((cp ^ (((l4 & 1) | (q & 2)) << 1)) << 4) - 1024 * j;
  }
  const char* wb = (const char*)((q & 2) ? wu : wg) + ft * 128;
  auto stage = [&](int k, u32 slot) {
    const int s = (k + s0) & 7;
    dma16x4(xb + s * 128, slot + q * 12288, vx[0], vx[1], vx[2], vx[3]);
    dma16x4(wb + s * (64 * 3584), slot + q * 12288 + 8192, vw[0], vw[1], vw[2], vw[3]);
    dma16x4(xb + s * 128 + 32768, slot + q * 12288 + 4096, vx[0], vx[1], vx[2], vx[3]);
  };
  if (lane < 8) *(LDS u32*)(sm + L_MISC + 48 + lane * 4) = 0u;
  LGKM0();
  BAR();
  if (w < 4 && !helper) stage(0, sb);
  SCHED();
  auto wtouch = [&](int i0, int i1) {
#pragma unroll 1
    for (int i = i0; i < i1; ++i) {
      const int c = (i * 4 + q + bq * 7) % 28;
      dma4((const char*)wd + (size_t)c * 65536 + a2 * 512 + (idx & 3) * 128, sb + L_DUMMY, lane * 1024);
    }
  };

  if (helper) {
    u32x4 xr[8];
#pragma unroll
    for (int j = 0; j < 8; ++j) xr[j] = *(const u32x4*)((const char*)x + (size_t)(hb * 64 + 32 * (w >> 2) + 4 * j + q) * 1024 + lane * 16);
    u64 tag;
    asm volatile("s_waitcnt vmcnt(8)\n\tv_mov_b64 %0, %1" : "=&v"(tag) : "v"(l_gen) : "memory");
    tag += TAG_STEP;
    float d[8][8], s[8];
#pragma unroll
    for (int j = 0; j < 8; ++j) {
#pragma unroll
      for (int e = 0; e < 4; ++e) { d[j][2 * e] = bflo(xr[j][e]); d[j][2 * e + 1] = bfhi(xr[j][e]); }
      const f32x2 t = (f32x2{d[j][0], d[j][1]} + f32x2{d[j][2], d[j][3]}) + (f32x2{d[j][4], d[j][5]} + f32x2{d[j][6], d[j][7]});
      s[j] = t.x + t.y;
    }
    asm volatile("" : "+v"(s[7]));
    STAMP(G_Hx);
    if (w < 4) wtouch(0, 1);
    auto red8 = [&](float (&v)[8], float (&u)[2]) {
      float t[4];
#pragma unroll
      for (int i = 0; i < 4; ++i) {
        auto r = __builtin_amdgcn_permlane32_swap(__builtin_bit_cast(u32, v[i]), __builtin_bit_cast(u32, v[i + 4]), false, false);
        t[i] = __builtin_bit_cast(float, (u32)r[0]) + __builtin_bit_cast(float, (u32)r[1]);
      }
#pragma unroll
      for (int i = 0; i < 2; ++i) {
        auto r = __builtin_amdgcn_permlane16_swap(__builtin_bit_cast(u32, t[i]), __builtin_bit_cast(u32, t[i + 2]), false, false);
        float z = __builtin_bit_cast(float, (u32)r[0]) + __builtin_bit_cast(float, (u32)r[1]);
        z += dppf<0x128>(z); z += dppf<0x124>(z); z += dppf<0x122>(z); z += dppf<0x121>(z);
        u[i] = z;
      }
    };
    auto bcast = [&](float (&u)[2], int j) { return __builtin_bit_cast(float, __builtin_amdgcn_readlane(__builtin_bit_cast(int, u[j & 1]), 16 * (j >> 1))); };
    float mean[2], var[2], rs[2], of[2];
    red8(s, mean);
    mean[0] *= (1.f / 512.f); mean[1] *= (1.f / 512.f);
#pragma unroll
    for (int j = 0; j < 8; ++j) {
      const float m = bcast(mean, j);
      f32x2 a2v = {0.f, 0.f};
#pragma unroll
      for (int e = 0; e < 4; ++e) {
        const f32x2 t = f32x2{d[j][2 * e], d[j][2 * e + 1]} - f32x2{m, m};
        d[j][2 * e] = t.x; d[j][2 * e + 1] = t.y;
        a2v = __builtin_elementwise_fma(t, t, a2v);
      }
      s[j] = a2v.x + a2v.y;
    }
    red8(s, var);
#pragma unroll
    for (int i = 0; i < 2; ++i) {
      const float mb = rbf(mean[i]);
      rs[i] = __builtin_amdgcn_rcpf(rbf(__builtin_amdgcn_sqrtf(rbf(rbf(var[i] * (1.f / 512.f)) + 1e-5f))));
      of[i] = (mean[i] - mb) * rs[i];
      mean[i] = mb;
    }
    if ((lane & 15) == 0) {
#pragma unroll
      for (int i = 0; i < 2; ++i) {
        const int row = 32 * (w >> 2) + 4 * (i + 2 * (lane >> 4)) + q;
        *(LDS float*)(sm + 65536 + row * 4) = mean[i];
        *(LDS float*)(sm + 65536 + 256 + row * 4) = rs[i];
      }
    }
#pragma unroll
    for (int j = 0; j < 8; ++j) {
      const float r = bcast(rs, j), o = bcast(of, j);
#pragma unroll
      for (int e = 0; e < 4; ++e) {
        const f32x2 t = __builtin_elementwise_fma(f32x2{d[j][2 * e], d[j][2 * e + 1]}, f32x2{r, r}, f32x2{o, o});
        d[j][2 * e] = t.x; d[j][2 * e + 1] = t.y;
      }
    }
#pragma unroll
    for (int i = 0; i < 8; ++i) {
      const u32x4 o = {pkh(d[0][i], d[1][i]), pkh(d[2][i], d[3][i]), pkh(d[4][i], d[5][i]), pkh(d[6][i], d[7][i])};
      *(LDS u32x4*)(sm + (8 * lane + i) * 128 + ((w ^ (lane & 7)) << 4)) = o;
    }
    LGKM0();
    STAMP(G_Hln);
    BAR();
    if (w == 0) {
      __builtin_amdgcn_raw_buffer_store_b32(*(LDS u32*)(sm + 65536 + lane * 4), wsr, lane * 4, WS_MEAN + hb * 256, 16);
      __builtin_amdgcn_raw_buffer_store_b32(*(LDS u32*)(sm + 65536 + 256 + lane * 4), wsr, lane * 4, WS_RSTD + hb * 256, 16);
    }
    u32x4 v[8];
#pragma unroll
    for (int it = 0; it < 8; ++it) v[it] = *(LDS u32x4*)(sm + (64 * w + 8 * it + (lane >> 3)) * 128 + (((lane & 7) ^ it) << 4));
#pragma unroll
    for (int it = 0; it < 8; ++it) st16_sc1(ws + WS_U + (64 * w + 8 * it) * 4096 + hb * 128, (lane >> 3) * 4096 + (lane & 7) * 16, v[it]);
    STAMP(G_Hst);
    VMCNT(0);
    BAR();
    if (threadIdx.x == 0) set_flag(ws, WS_FS + hb * 128, tag);
    STAMP(G_Hflag);
  }
  SCHED();

  if (w < 4) {
    if (!helper) {
      const int mh = w >> 1, fp = w & 1;
      const u32 r31 = lane & 31, qq = (lane & 15) >> 2, gu = (lane >> 4) & 1, p4 = lane & 3;
      LDS char* xa[3][4];
      LDS char* wa[3][2];
#pragma unroll
      for (int s = 0; s < 3; ++s) {
#pragma unroll
        for (int ks = 0; ks < 4; ++ks) {
          xa[s][ks] = sm + s * SLOT + mh * 24576 + r31 * 128 + (((2 * ks + h) ^ ((r31 >> 1) & 7)) << 4);
          asm volatile("" : "+v"(xa[s][ks]));
        }
#pragma unroll
        for (int j = 0; j < 2; ++j) {
          wa[s][j] = sm + s * SLOT + 8192 + gu * 24576 + (8 * h + qq) * 128 + (((2 * fp + (j ^ mh)) ^ (((qq >> 1) & 1) | (gu << 1))) << 5) + (((p4 & 1) << 1) | (p4 >> 1)) * 8;
          asm volatile("" : "+v"(wa[s][j]));
        }
      }
      const u32 pq = (lane >> 2) & 3;
      LDS char* csw = sm + L_CS + (2 * fp + mh) * 128 + gu * 64 + (((pq & 1) << 1) | (pq >> 1)) * 16 + p4 * 4;
      LDS char* csr[2] = {sm + L_CS + (2 * fp + mh) * 128 + h * 32, sm + L_CS + (2 * fp + (mh ^ 1)) * 128 + h * 32};
      LDS char* mur = sm + L_MU + (mh * 128 + r31) * 4;
      const char* hb0 = ws + WS_H + (((mt * 8 + mh * 4) * 28 + ft) * 4 + 2 * fp) * 1024;
      u32 hv[2] = {(u32)(lane * 16 + mh * 1024), (u32)(lane * 16 + (mh ^ 1) * 1024)};
      asm volatile("" : "+v"(csw), "+v"(csr[0]), "+v"(csr[1]), "+v"(mur), "+v"(hv[0]), "+v"(hv[1]));
      stage(1, sb + SLOT);
      STAMP(G_issued);
      f32x16 acc[4][2];
      f32x16 zero;
#pragma unroll
      for (int e = 0; e < 16; ++e) zero[e] = 0.f;
      u32x4 xf[2][4];
      u32x2 wf[2][2][2];
      float cs = 0.f;
      auto rd = [&](int buf, int g, int ks, int sl, int i0) {
        if (g < 4) wf[buf][g >> 1][g & 1] = __builtin_bit_cast(u32x2, __builtin_amdgcn_ds_read_tr16_b64_v4i16((LDS s16x4*)(wa[sl][g >> 1] + (ks >> 1) * 12288 + (ks & 1) * 2048 + (g & 1) * 512)));
        else { const int i = i0 + g - 4; xf[buf][g - 4] = *(LDS u32x4*)(xa[sl][ks] + (i >> 1) * 12288 + (i & 1) * 4096); }
      };
      auto csum = [&](int buf, int e) { const u32 v = wf[buf][0][e >> 2][(e >> 1) & 1]; cs += (e & 1) ? bfhi(v) : bflo(v); };
      auto wfrag = [&](int c, int j) { return u32x4{wf[c][j][0].x, wf[c][j][0].y, wf[c][j][1].x, wf[c][j][1].y}; };
      asm volatile("s_waitcnt vmcnt(12)" ::"v"(l_lar), "v"(l_aim), "v"(l_cr), "v"(l_ci), "v"(l_ldt), "v"(l_gen) : "memory");
      lds_inc(sm, L_MISC + 56, lane);
      if (w == 3) dma4(xtouch + ((s0 + 2 + ((idx >> 2) & 1)) & 7) * 128, sb + L_DUMMY, lane * 1024);
      wait_lds_cnt(sm, L_MISC + 56, 4);
      STAMP(G_st0);
#pragma unroll
      for (int g = 0; g < 8; ++g) rd(0, g, 0, 0, 0);
      SCHED();
#pragma unroll
      for (int st = 0; st < 24; ++st) {
        const int c = st & 1, n = c ^ 1, k = st >> 2, ks = st & 3;
        const bool last = ks == 3;
        const int nsl = ((st + 1) >> 2) % 3, nks = (st + 1) & 3, nr = st == 23 ? 6 : 8;
#pragma unroll
        for (int g = 0; g < 8; ++g) {
          if (last && g == 2) {
            if (k == 0) { if (w == 3) VMCNT(1); else VMCNT(0); }
            __builtin_amdgcn_s_waitcnt(0xC07F); BAR();
            if (k == 0) STAMP(G_B1);
            if (k == 1) STAMP(G_B2);
            if (k == 3) STAMP(G_B4);
            if (k == 5) STAMP(G_B6);
            SCHED();
          }
          acc[g >> 1][g & 1] = mma_bf(wfrag(c, g & 1), xf[c][g >> 1], st == 0 ? zero : acc[g >> 1][g & 1]);
          SCHED();
          if (!last) rd(n, g, nks, nsl, 0);
          else if (g >= 2) {
            if (g < 4) { rd(n, 2 * g - 4, nks, nsl, 0); rd(n, 2 * g - 3, nks, nsl, 0); }
            else if (g < nr) rd(n, g, nks, nsl, 0);
          }
          csum(c, g);
          SCHED();
        }
      }
      float mu[4];
      f32x4 cg[2][2], cu[2][2];
      u32x4 ho;
      auto epi = [&](int i, int j, int e) {
        const float g0 = fmaf(cg[j][e >> 1][(2 * e) & 3], mu[i], acc[i][j][2 * e]), g1 = fmaf(cg[j][e >> 1][(2 * e + 1) & 3], mu[i], acc[i][j][2 * e + 1]);
        const float u0 = fmaf(cu[j][e >> 1][(2 * e) & 3], mu[i], acc[i][j][8 + 2 * e]), u1 = fmaf(cu[j][e >> 1][(2 * e + 1) & 3], mu[i], acc[i][j][9 + 2 * e]);
        ho[e] = pkbf(g0 * u0, g1 * u1);
        if (e == 3) st16_sc1(hb0 + i * (28 * 4096), hv[j], ho);
      };
#pragma unroll
      for (int st = 0; st < 16; ++st) {
        const int c = st & 1, n = c ^ 1, pr = st >> 3;
        const int nst = st + 1, nsl = (nst >> 2) & 1, nks = nst & 3, npr = nst >> 3;
        const bool bar = st == 3 || st == 9 || st == 11 || st == 15;
#pragma unroll
        for (int g = 0; g < 4; ++g) {
          if (bar && g == 1) {
            __builtin_amdgcn_s_waitcnt(0xC07F); BAR();
            STAMP(st == 3 ? G_B7 : st == 9 ? G_B8 : st == 11 ? G_B9 : G_B10);
            SCHED();
          }
          const int i = 2 * pr + (g >> 1), j = g & 1;
          acc[i][j] = mma_bf(wfrag(c, j), xf[c][g >> 1], acc[i][j]);
          SCHED();
          const int gg = bar ? g - 1 : g;
          if (st < 15 && gg >= 0 && gg < 3) {
            constexpr int ord[6] = {0, 1, 4, 2, 3, 5};
            rd(n, ord[2 * gg], nks, nsl, 2 * npr); rd(n, ord[2 * gg + 1], nks, nsl, 2 * npr);
          }
          if (st < 8) { csum(c, 2 * g); csum(c, 2 * g + 1); }
          if (st == 8 && g == 0) {
            auto r = __builtin_amdgcn_permlane32_swap(__builtin_bit_cast(u32, cs), __builtin_bit_cast(u32, cs), false, false);
            *(LDS float*)csw = __builtin_bit_cast(float, (u32)r[0]) + __builtin_bit_cast(float, (u32)r[1]);
          }
          if (st == 8 && (g == 1 || g == 2)) {
            mu[2 * g - 2] = -*(LDS float*)(mur + (2 * g - 2) * 128);
            mu[2 * g - 1] = -*(LDS float*)(mur + (2 * g - 1) * 128);
          }
          const int t = st * 4 + g - 37;
          if (t >= 0 && t < 4) {
            cg[t >> 1][t & 1] = *(LDS f32x4*)(csr[t >> 1] + (t & 1) * 16);
            cu[t >> 1][t & 1] = *(LDS f32x4*)(csr[t >> 1] + 64 + (t & 1) * 16);
          }
          if (t >= 4) {
            const int v = t - 4;
            if (v % 3 != 2 && v - v / 3 < 16) { const int pc = v - v / 3; epi(pc >> 3, (pc >> 2) & 1, pc & 3); }
          }
          SCHED();
        }
      }
#pragma unroll
      for (int i = 2; i < 4; ++i)
#pragma unroll
        for (int j = 0; j < 2; ++j) {
          u32x4 o;
#pragma unroll
          for (int e = 0; e < 4; ++e) {
            const f32x2 m2 = {mu[i], mu[i]};
            const f32x2 g2 = __builtin_elementwise_fma(f32x2{cg[j][e >> 1][(2 * e) & 3], cg[j][e >> 1][(2 * e + 1) & 3]}, m2, f32x2{acc[i][j][2 * e], acc[i][j][2 * e + 1]});
            const f32x2 u2 = __builtin_elementwise_fma(f32x2{cu[j][e >> 1][(2 * e) & 3], cu[j][e >> 1][(2 * e + 1) & 3]}, m2, f32x2{acc[i][j][8 + 2 * e], acc[i][j][9 + 2 * e]});
            const f32x2 h2 = g2 * u2;
            o[e] = pkbf(h2.x, h2.y);
          }
          st16_sc1(hb0 + i * (28 * 4096), hv[j], o);
        }
      STAMP(G_hstore);
    } else {
      wtouch(1, 7);
      STAMP(G_Htouch);
#pragma unroll
      for (int i = 0; i < 10; ++i) BAR();
    }

    const u64 tag = *(LDS u64*)(sm + L_MISC);
    const float ysc = 2.f * *(LDS float*)(sm + L_MISC + 40 + chl * 4) *
                      fmaxf(*(LDS float*)(sm + L_MISC + 8 + chl * 8), *(LDS float*)(sm + L_MISC + 12 + chl * 8));
    const int t = threadIdx.x, sr = t >> 3, dg = t & 7;
    const int rowA = 128 * B128 + 32 * sh + sr;
    u32x4 xA, xB, dv;
    float mm[2], rr[2];
    {
      f32x16 z[4];
      const int ps = w & 1;
      LDS char* kp = sm + L_KA + chl * 4160 + 64 + (32 * h + 16 * ((lane >> 4) & 1) + 4 * (lane & 3) - 4 * ((lane & 15) >> 2)) * 2;
      LDS char* up = sm + L_U + chl * 4608 + 512 + (lane & 31) * 16 - 64 * h;
      const u32 fh = WS_FH + ((mt * 4 + w) * 32 + ft) * 128;
      auto early = [&]() {
        VMCNT(4);
        if (lane == 0 && !helper) set_flag(ws, fh, tag);
        STAMP(G_hflagA);
      };
      auto mid = [&]() {
        VMCNT(0);
        if (lane == 0 && !helper) set_flag(ws, fh + 1024 * 128, tag);
        STAMP(G_hflagB);
        xA = *(const u32x4*)((const char*)x + (size_t)rowA * 1024 + ct * 128 + dg * 16);
        xB = *(const u32x4*)((const char*)x + (size_t)(rowA + 64) * 1024 + ct * 128 + dg * 16);
        dv = *(const u32x4*)((const char*)dsk + ct * 128 + dg * 16);
#pragma unroll
        for (int m = 0; m < 2; ++m) {
          mm[m] = __builtin_bit_cast(float, __builtin_amdgcn_raw_buffer_load_b32(wsr, (rowA + 64 * m) * 4, WS_MEAN, 16));
          rr[m] = __builtin_bit_cast(float, __builtin_amdgcn_raw_buffer_load_b32(wsr, (rowA + 64 * m) * 4, WS_RSTD, 16));
        }
      };
      if (ps) conv_mm<1>(kp, up, z, early, mid); else conv_mm<0>(kp, up, z, early, mid);
      STAMP(G_convmm);
      int T[4];
      T[0] = ps; T[1] = 3 - ps; T[2] = 4 + ps; T[3] = 7 - ps;
      LDS char* car = sm + L_CAR + chl * 528;
#pragma unroll
      for (int i = 0; i < 4; ++i)
#pragma unroll
        for (int g = 0; g < 4; ++g) {
          z[i][4 * g + 2] += dppf<0x138>(z[i][4 * g + 3]);
          z[i][4 * g + 1] += dppf<0x138>(z[i][4 * g + 2]);
        }
      if (lane == 63) {
#pragma unroll
        for (int i = 0; i < 4; ++i)
#pragma unroll
          for (int g = 0; g < 4; ++g)
            *(LDS f32x4*)(car + (4 * T[i] + g + 1) * 16) = f32x4{z[i][4 * g], z[i][4 * g + 1], z[i][4 * g + 2], z[i][4 * g + 3]};
      }
      SCHED();
#pragma unroll
      for (int i = 0; i < 4; ++i)
#pragma unroll
        for (int g = 0; g < 4; ++g) z[i][4 * g] += dppf<0x138>(z[i][4 * g + 1]);
      LGKM0();
      lds_inc(sm, L_MISC + 48 + chl * 4, lane);
      wait_lds_cnt(sm, L_MISC + 48 + chl * 4, 2);
      STAMP(G_carry);
      if (lane < 3) {
#pragma unroll
        for (int i = 0; i < 4; ++i)
#pragma unroll
          for (int g = 0; g < 4; ++g) z[i][4 * g] += *(LDS float*)(car + (4 * T[i] + g) * 16 + 4 + lane * 4);
      }
#pragma unroll
      for (int i = 0; i < 4; ++i)
#pragma unroll
        for (int g = 0; g < 2; ++g)
          __builtin_amdgcn_raw_buffer_store_b32(pkbf(z[i][8 * g] * ysc, z[i][8 * g + 4] * ysc), wsr, h * 65536 + (lane & 31) * 4,
                                                WS_Y + (2 * T[i] + g) * 131072 + ch * 128, 16);
    }
    STAMP(G_ystore);
    VMCNT(0);
    if (lane == 0) set_flag(ws, WS_FY + ((w & 1) * 512 + ch) * 128, tag);
    STAMP(G_yflag);
    float base[2][8], r2[2];
#pragma unroll
      for (int m = 0; m < 2; ++m) {
        r2[m] = rr[m] * rr[m];
#pragma unroll
        for (int e = 0; e < 4; ++e) {
          const u32 xv = m ? xB[e] : xA[e];
          base[m][2 * e] = fmaf((bflo(xv) - mm[m]) * rr[m], bflo(dv[e]), bflo(xv));
          base[m][2 * e + 1] = fmaf((bfhi(xv) - mm[m]) * rr[m], bfhi(dv[e]), bfhi(xv));
        }
      }
#pragma unroll
    for (int e = 0; e < 8; ++e) asm volatile("" : "+v"(base[0][e]), "+v"(base[1][e]));
    {
      const int cti = B128 >> 1, psn = (cti ^ (cti >> 1)) & 1;
      wait_flags(ws, WS_FY + (psn * 512 + ct * 64 + 16 * w + (lane & 15)) * 128, true, tag);
    }
    STAMP(G_ypoll);
    {
      const u32 yo = WS_Y + B128 * 131072 + sh * 65536 + (ct * 64 + 16 * w) * 128;
      const u32x4 y0 = __builtin_amdgcn_raw_buffer_load_b128(wsr, lane * 16, yo, 16);
      const u32x4 y1 = __builtin_amdgcn_raw_buffer_load_b128(wsr, lane * 16, yo + 1024, 16);
      LDS char* yt = sm + L_KA + chl * 4160;
      const u32 cl = (w & 1) * 16 + (lane >> 3), sq = lane & 7;
#pragma unroll
      for (int e = 0; e < 4; ++e) {
        *(LDS u32*)(yt + (4 * sq + e) * 128 + (((cl >> 2) ^ sq) << 4) + (cl & 3) * 4) = y0[e];
        *(LDS u32*)(yt + (4 * sq + e) * 128 + ((((cl + 8) >> 2) ^ sq) << 4) + (cl & 3) * 4) = y1[e];
      }
    }
    LGKM0();
    lds_inc(sm, L_MISC + 60, lane);
    STAMP(G_yld);
    u32x4 yv[2];
#pragma unroll
    for (int m = 0; m < 2; ++m) {
      if (m == 0) wait_lds_cnt(sm, L_MISC + 60, 8); else BAR();
      STAMP(m ? G_BB : G_A);
      if (m == 0) {
        LDS char* yt = sm + L_KA + (dg >> 2) * 4160 + sr * 128;
        yv[0] = *(LDS u32x4*)(yt + (((2 * (dg & 3)) ^ (sr >> 2)) << 4));
        yv[1] = *(LDS u32x4*)(yt + (((2 * (dg & 3) + 1) ^ (sr >> 2)) << 4));
      }
      f32x4 p[4][2];
#pragma unroll
      for (int k = 0; k < 4; ++k) {
        const u32 pb = m == 0 ? (k < 2 ? L_PA01 + k * 8192 : L_PA23 + (k - 2) * 8192) : 2 * SLOT + k * 8192;
#pragma unroll
        for (int e = 0; e < 2; ++e) p[k][e] = *(LDS f32x4*)(sm + pb + sr * 256 + (((2 * dg + e) ^ (sr & 7)) << 4));
      }
      u32x4 o;
#pragma unroll
      for (int e = 0; e < 4; ++e) {
        float v[2];
#pragma unroll
        for (int u = 0; u < 2; ++u) {
          const int c = 2 * e + u;
          const float s = (p[0][c >> 2][c & 3] + p[1][c >> 2][c & 3]) + (p[2][c >> 2][c & 3] + p[3][c >> 2][c & 3]);
          const u32 yw = yv[c >> 2][c & 3];
          v[u] = fmaf(s, r2[m], base[m][c] + (m ? bfhi(yw) : bflo(yw)));
        }
        o[e] = pkbf(v[0], v[1]);
      }
      asm volatile("global_store_dwordx4 %0, %1, %2 sc0 sc1\n\ts_nop 1" ::"v"((u32)((rowA + 64 * m) * 1024 + ct * 128 + dg * 16)), "v"(o), "s"(out) : "memory");
      if (m == 0) STAMP(G_Adone);
      if (m == 0 && b == 255 && w == 0) {
#pragma unroll 1
        for (int i = 0; i < 4; ++i) wait_flags(ws, WS_FY + (i * 128 + lane * 2) * 128, true, tag);
        if (lane == 0) set_flag(ws, WS_GEN, tag);
      }
    }
  } else {
    u32 p_lar, p_aim, p_cr, p_ci, p_ldt;
    u64 gen;
    asm volatile("s_waitcnt vmcnt(0)\n\tv_lshlrev_b32 %0, 16, %6\n\tv_lshlrev_b32 %1, 16, %7\n\tv_lshlrev_b32 %2, 16, %8\n\t"
                 "v_lshlrev_b32 %3, 16, %9\n\tv_lshlrev_b32 %4, 16, %10\n\tv_mov_b64 %5, %11"
                 : "=&v"(p_lar), "=&v"(p_aim), "=&v"(p_cr), "=&v"(p_ci), "=&v"(p_ldt), "=&v"(gen)
                 : "v"(l_lar), "v"(l_aim), "v"(l_cr), "v"(l_ci), "v"(l_ldt), "v"(l_gen) : "memory");
    const u64 tag = gen + TAG_STEP;
    __builtin_amdgcn_s_setprio(3);
    {
      float dt = rbf(__expf(__builtin_bit_cast(float, p_ldt)));
      STAMP(S_params);
      const float zr = -rbf(__expf(__builtin_bit_cast(float, p_lar))) * dt * 1.44269504f, zi = __builtin_bit_cast(float, p_aim) * dt * 0.15915494f;
      auto cexp = [&](float p, float sg) {
        const float mg = __builtin_amdgcn_exp2f(zr * p), t = __builtin_amdgcn_fractf(zi * p);
        return f32x2{mg * __builtin_amdgcn_cosf(t), sg * mg * __builtin_amdgcn_sinf(t)};
      };
      u32 mx = max(p_cr & 0x7fffffffu, p_ci & 0x7fffffffu);
      mx = max(mx, dppu<0x121>(mx)); mx = max(mx, dppu<0x122>(mx)); mx = max(mx, dppu<0x124>(mx)); mx = max(mx, dppu<0x128>(mx));
      u32 ex = max(max(__builtin_amdgcn_readlane(mx, 0), __builtin_amdgcn_readlane(mx, 16)), max(__builtin_amdgcn_readlane(mx, 32), __builtin_amdgcn_readlane(mx, 48)));
      ex = min(max(ex >> 23, 1u), 253u);
      const float isc = __builtin_bit_cast(float, (254u - ex) << 23);
      LDS char* sc = sm + 2 * SLOT + q * 12288;
      LDS char* sx = sm + L_KX + q * 1280 - 48 * 256;
      u32 fv[53];
      auto put = [&](int row, f32x2 v) { fv[row] = pkh(v.x, v.y); };
      const f32x2 w8 = cexp(8.f, 1.f), w64 = cexp(64.f, 1.f), w1 = cexp(1.f, -1.f), w256 = cexp(256.f, -1.f);
      f32x2 P[8], Q[4];
      P[0] = f32x2{1.f, 0.f}; P[1] = w8; P[2] = cmul(w8, w8); P[3] = cmul(P[2], w8); P[4] = cmul(P[2], P[2]);
      P[5] = cmul(P[1], P[4]); P[6] = cmul(P[2], P[4]); P[7] = cmul(P[3], P[4]);
      Q[1] = w64; Q[2] = cmul(w64, w64); Q[3] = cmul(Q[2], w64);
#pragma unroll
      for (int j = 0; j < 8; ++j) {
#pragma unroll
        for (int i = 0; i < 4; ++i) put(8 * i + j, i == 0 ? P[j] : j == 0 ? Q[i] : cmul(P[j], Q[i]));
      }
      f32x2 A[8];
      A[0] = f32x2{__builtin_bit_cast(float, p_cr) * isc, -__builtin_bit_cast(float, p_ci) * isc};
      const f32x2 w2 = cmul(w1, w1), w4 = cmul(w2, w2), w512 = cmul(w256, w256);
      A[1] = cmul(A[0], w1); A[2] = cmul(A[0], w2); A[3] = cmul(A[1], w2);
#pragma unroll
      for (int j = 0; j < 4; ++j) A[4 + j] = cmul(A[j], w4);
      const f32x2 E1 = cmul(A[0], w256), F1 = cmul(A[4], w256);
      const f32x2 E2 = cmul(A[0], w512), E3 = cmul(E1, w512);
      put(32, A[0]); put(40, E1);
      put(33, A[1]); put(41, cmul(A[2], w256));
      put(34, A[2]); put(42, F1);
      put(35, A[3]); put(43, cmul(A[6], w256));
      put(36, A[4]); put(44, E2);
      put(37, A[5]); put(45, cmul(A[4], w512));
      put(38, A[6]); put(46, E3);
      put(39, A[7]); put(47, cmul(F1, w512));
      const f32x2 E4 = cmul(E2, w512), E5 = cmul(E3, w512), E6 = cmul(E4, w512);
      put(48, E4); put(49, E5); put(50, E6); put(51, cmul(E5, w512)); put(52, cmul(E6, w512));
#pragma unroll
      for (int r = 0; r < 52; r += 4) asm volatile("" : "+v"(fv[r]), "+v"(fv[r + 1]), "+v"(fv[r + 2]), "+v"(fv[r + 3]));
      STAMP(S_valu);
#pragma unroll
      for (int r = 0; r < 53; ++r) {
        const int row = r < 48 ? (r >> 4) * 16 + ((r & 15) >> 1) + (r & 1) * 8 : r;
        *(LDS u32*)((row < 48 ? sc : sx) + row * 256 + ((lane * 4) ^ ((row & 7) << 4))) = fv[row];
      }
      f32x16 kk;
#pragma unroll
      for (int e = 0; e < 16; ++e) kk[e] = 0.f;
      const u32 ra = (lane & 31) * 256 + ((h ^ (lane & 7)) << 4);
      LDS char* la = ((lane & 16) ? sx : sc) + 8192;
      u32x4 fa[8], fb[8];
#pragma unroll
      for (int s = 0; s < 8; ++s) {
        const u32 o = (ra ^ ((s & 3) << 5)) + (s >> 2) * 128;
        fa[s] = *(LDS u32x4*)(la + o);
        fb[s] = *(LDS u32x4*)(sc + o);
      }
      LGKM0();
      SCHED();
      STAMP(S_factors);
      __builtin_amdgcn_s_setprio(0);
      if (!helper) stage(2, sb + 2 * SLOT);
      STAMP(S_st2);
      BAR();
      if (!helper) stage(3, sb);
      SCHED();
#pragma unroll
      for (int s = 0; s < 8; ++s) kk = mma_h(fa[s], fb[s], kk);
      LDS char* pp = sm + L_KA + q * 2688;
      const u32 c31 = lane & 31;
      *(LDS f32x4*)(pp + c31 * 32 + h * 16) = f32x4{kk[0], kk[1], kk[2], kk[3]};
      *(LDS f32x2*)(pp + (h ? 1536 + c31 * 8 : 1024 + c31 * 16)) = f32x2{kk[4], kk[5]};
      *(LDS f32x2*)(pp + (h ? 1792 + c31 * 8 : 1032 + c31 * 16)) = f32x2{kk[6], kk[7]};
      *(LDS float*)(pp + 2048 + h * 512 + c31 * 4) = kk[8];
      if (lane < 32) {
#pragma unroll
        for (int e = 9; e < 12; ++e) *(LDS float*)(pp + 2048 + (e - 8) * 128 + c31 * 4) = kk[e];
      }
      if (lane == 0) {
        *(LDS u32*)(sm + L_MISC + 8 + q * 4) = ex << 23;
        *(LDS float*)(sm + L_MISC + 40 + chl * 4) = dt;
        *(LDS u64*)(sm + L_MISC) = tag;
      }
      STAMP(S_kgen);
    }
    u32 vd[4];
#pragma unroll
    for (int j = 0; j < 4; ++j) vd[j] = (j >> 1) * 65536 + ((j & 1) * 8 + l3) * 1024 + ((cp ^ ((l4 & 1) << 2)) << 4) - 1024 * j;
    const char* db = (const char*)wd + q * 16384 + ct * 128;
    auto wd2 = [&](int j, u32 dst) { dma16x4(db + j * 65536, sb + dst, vd[0], vd[1], vd[2], vd[3]); };
    const u32 pollz = L_POLL + q * 256;
    const u32 tagw = h ? (u32)(tag >> 32) : (u32)tag;
    VMCNT(12); LGKM0();
    BAR();
    if (!helper) stage(4, sb + SLOT);
    u32 tp0, tp1; u32x2 tp2; u32x4 tp3;
    const u32 tt = (q & 1) * 64 + lane;
    {
      const float e0 = *(LDS float*)(sm + L_MISC + 8 + chl * 8), e1 = *(LDS float*)(sm + L_MISC + 12 + chl * 8);
      const float ie = __builtin_amdgcn_rcpf(fmaxf(e0, e1));
      const f32x2 f0 = {e0 * ie, e0 * ie}, f1 = {e1 * ie, e1 * ie};
      LDS char* pa = sm + L_KA + chl * 5376;
      u32 pk[4];
#pragma unroll
      for (int r = 0; r < 4; ++r) {
        const u32 o = r == 0 ? tt * 8 : 512 + r * 512 + tt * 4;
        const f32x2 a = {*(LDS float*)(pa + o), *(LDS float*)(pa + o + 4)};
        const f32x2 c = {*(LDS float*)(pa + 2688 + o), *(LDS float*)(pa + 2688 + o + 4)};
        const f32x2 v = __builtin_elementwise_fma(a, f0, c * f1);
        pk[r] = pkh(v.x, v.y);
      }
      const bool ev = (lane & 1) == 0;
      auto lo2 = [](u32 v) { return __builtin_amdgcn_perm(v, v, 0x01000100); };
      auto hi2 = [](u32 v) { return __builtin_amdgcn_perm(v, v, 0x03020302); };
      tp0 = pk[0];
      tp1 = ev ? lo2(pk[1]) : pk[1];
      tp2 = u32x2{lo2(pk[2]), ev ? pk[2] : hi2(pk[2])};
      tp3 = u32x4{lo2(pk[3]), lo2(pk[3]), ev ? pk[3] : hi2(pk[3]), hi2(pk[3])};
      asm volatile("" : "+v"(tp0), "+v"(tp1), "+v"(tp2), "+v"(tp3));
    }
    VMCNT(12); LGKM0();
    BAR();
    if (!helper) stage(5, sb + 2 * SLOT);
    {
      LDS char* ka = sm + L_KA + chl * 4160 + 64;
      *(LDS u32*)(ka + tt * 4) = tp0;
      *(LDS u32*)(ka + 512 + tt * 4) = tp1;
      *(LDS u32x2*)(ka + 1024 + tt * 8) = tp2;
      *(LDS u32x4*)(ka + 2048 + tt * 16) = tp3;
      if (tt < 16) *(LDS u32*)(ka - 64 + tt * 4) = 0u;
      if (tt < 4) *(LDS u32*)(sm + L_CAR + chl * 528 + tt * 4) = 0u;
    }
    VMCNT(12); LGKM0();
    BAR();
    if (!helper) { dma4_sc1(ws + WS_FS + (mt * 4 + q) * 128, sb + pollz, h * 4); stage(6, sb); }
    VMCNT(12);
    BAR();
    if (!helper) {
      if (!__all(*(LDS u32*)(sm + pollz + lane * 4) == tagw)) wait_flags(ws, WS_FS + (mt * 4 + q) * 128, true, tag);
      dma4_sc1(ws + WS_MEAN + (mt * 256 + q * 64) * 4, sb + L_MU + q * 256, lane * 4);
      stage(7, sb + SLOT);
    }
    VMCNT(12);
    STAMP(S_aB6);
    BAR();
    const u32 r2 = 2 * SLOT + q * 8192, r0 = q * 12288, r1 = SLOT + q * 8192;
    dma4_sc1(ws + WS_FS, sb + pollz, (lane & 31) * 128 + h * 4);
    wd2(0, r2); wd2(2, r2 + 4096);
    if (tt < 32) *(LDS u32x4*)(sm + L_U + chl * 4608 + tt * 16) = u32x4{0u, 0u, 0u, 0u};
    VMCNT(8);
    STAMP(S_aB7);
    BAR();
    if (!__all(*(LDS u32*)(sm + pollz + lane * 4) == tagw)) wait_flags(ws, WS_FS + (lane & 31) * 128, true, tag);
    STAMP(S_uchk);
    dma16x2_sc1(ws + WS_U + b * 8192 + q * 2048, sb + L_U + 512 + chl * 4608 + (q & 1) * 2048, lane * 16);
    const u32 qq = (lane & 15) >> 2;
    const u32 ta = (8 * h + qq) * 128 + (l4 & 1) * 32 + (lane & 3) * 8;
    LDS char* tb[2][2];
#pragma unroll
    for (int n = 0; n < 2; ++n) {
      tb[0][n] = sm + r0 + ta + ((((qq >> 1) & 1) ^ n) << 6);
      tb[1][n] = sm + r1 + ta + ((((qq >> 1) & 1) ^ n) << 6);
      asm volatile("" : "+v"(tb[0][n]), "+v"(tb[1][n]));
    }
    u32x4 wr[14][2];
    auto grab = [&](int j, u32 o) {
#pragma unroll
      for (int e = 0; e < 2; ++e)
#pragma unroll
        for (int n = 0; n < 2; ++n) wr[j + e][n] = trpair(tb[1][n] + SLOT + o + e * 2048, tb[1][n] + SLOT + o + e * 2048 + 512);
      LGKM0();
    };
    VMCNT(6); grab(0, 0);         wd2(4, r2);
    VMCNT(6); grab(2, 4096);  wd2(6, r2 + 4096);
    STAMP(S_aB8);
    BAR();
    VMCNT(4); grab(4, 0);         wd2(8, r2);
    VMCNT(4); grab(6, 4096);  wd2(10, r2 + 4096);
    STAMP(S_aB9);
    BAR();
    VMCNT(4); wd2(18, r0);         grab(8, 0);
    VMCNT(4); wd2(12, r2);         grab(10, 4096);
    STAMP(S_aB10);
    BAR();
    VMCNT(4); wd2(16, r2 + 4096);
    VMCNT(4); wd2(20, r0 + 4096);  grab(12, 0);
    VMCNT(4); wd2(14, r2);
    VMCNT(4); wd2(22, r0 + 8192);
    const u32 fh = WS_FH + (B128 * 2 + (q >> 1)) * 4096;
    const u32 fl = min(lane & 31, 27) * 128;
    VMCNT(4); dma4_sc1(ws + fh, sb + pollz, fl + h * 4); wd2(24, r1);
    VMCNT(5); wd2(26, r1 + 4096);
    STAMP(S_Wdone);
    VMCNT(8);
    if (!__all(*(LDS u32*)(sm + pollz + lane * 4) == tagw)) wait_flags(ws, fh + fl, true, tag);
    STAMP(S_hpollA);
    constexpr int PF = 9;
    u32x4 hf[56];
    const u32 hoff = WS_H + ((4 * B128 + sh) * 112 + q) * 1024;
    auto hload = [&](int i) {
      hf[i] = __builtin_amdgcn_raw_buffer_load_b128(wsr, lane * 16, hoff + (i / 28) * (2 * 112 * 1024) + (i % 28) * 4096, 16);
    };
#pragma unroll
    for (int i = 0; i < PF; ++i) { hload(i); SCHED(); }
    const u32 pw = (lane & 31) * 256;
#pragma unroll
    for (int m = 0; m < 2; ++m) {
      f32x16 acc0, acc1;
#pragma unroll
      for (int e = 0; e < 16; ++e) { acc0[e] = 0.f; acc1[e] = 0.f; }
      u32x4 wl[2][2];
#pragma unroll
      for (int j = 0; j < 28; ++j) {
        const int i = m * 28 + j;
        if (j + 1 >= 14 && j + 1 < 28) {
          const int u = j + 1 - 14;
          const u32 o = u < 4 ? SLOT + u * 2048 : u < 10 ? (u - 4) * 2048 : (u - 10) * 2048;
          const int rg = u >= 4 && u < 10 ? 0 : 1;
          wl[(j + 1) & 1][0] = trpair(tb[rg][0] + o, tb[rg][0] + o + 512);
          wl[(j + 1) & 1][1] = trpair(tb[rg][1] + o, tb[rg][1] + o + 512);
        }
        SCHED();
        acc0 = mma_bf(j < 14 ? wr[j < 14 ? j : 0][0] : wl[j & 1][0], hf[i], acc0);
        SCHED();
        if (i == 8) STAMP(S_h8);
        if (i + PF == 28 - PF - 1) dma4_sc1(ws + fh + 1024 * 128, sb + pollz, fl + h * 4);
        if (i + PF == 28) {
          if (!__all(*(LDS u32*)(sm + pollz + lane * 4) == tagw)) wait_flags(ws, fh + 1024 * 128 + fl, true, tag);
          STAMP(S_pollB);
        }
        if (i + PF < 56) hload(i + PF);
        SCHED();
        acc1 = mma_bf(j < 14 ? wr[j < 14 ? j : 0][1] : wl[j & 1][1], hf[i], acc1);
        SCHED();
      }
      STAMP(m ? S_Bmm : S_Amm);
      u32 pb;
      if (m == 0) {
        pb = q < 2 ? L_PA01 + q * 8192 : L_PA23 + (q - 2) * 8192;
        wait_lds_cnt(sm, L_MISC + 48, 2);
        wait_lds_cnt(sm, L_MISC + 52, 2);
      } else pb = r2;
#pragma unroll
      for (int g = 0; g < 4; ++g) {
        *(LDS f32x4*)(sm + pb + pw + (((2 * g + h) ^ (lane & 7)) << 4)) = f32x4{acc0[4 * g], acc0[4 * g + 1], acc0[4 * g + 2], acc0[4 * g + 3]};
        *(LDS f32x4*)(sm + pb + pw + 128 + (((2 * g + h) ^ (lane & 7)) << 4)) = f32x4{acc1[4 * g], acc1[4 * g + 1], acc1[4 * g + 2], acc1[4 * g + 3]};
      }
      LGKM0();
      STAMP(m ? S_B : S_A);
      if (m == 0) lds_inc(sm, L_MISC + 60, lane); else BAR();
    }
    STAMP_OUT();
    __builtin_amdgcn_endpgm();
  }
  STAMP_OUT();
}

extern "C" void kernel_launch(void* const* d_in, const int* in_sizes, int n_in, void* d_out, int out_size, void* d_ws, size_t ws_size,
                              hipStream_t stream) {
  fused_kernel<<<256, 512, 0, stream>>>((const u16*)d_in[0], (const u16*)d_in[1], (const u16*)d_in[2], (const u16*)d_in[3], (const u16*)d_in[4],
                                        (const u16*)d_in[5], (const u16*)d_in[6], (const u16*)d_in[8], (const u16*)d_in[9], (const u16*)d_in[10],
                                        (u16*)d_out, (char*)d_ws);
}
```

```cpp
#include <hip/hip_runtime.h>
#include <hip/hip_bf16.h>

typedef unsigned u32;
typedef unsigned short u16;
typedef unsigned long long u64;
typedef u32 u32x2 __attribute__((ext_vector_type(2)));
typedef u32 u32x4 __attribute__((ext_vector_type(4)));
typedef float f32x2 __attribute__((ext_vector_type(2)));
typedef float f32x4 __attribute__((ext_vector_type(4)));
typedef float f32x16 __attribute__((ext_vector_type(16)));
typedef short s16x4 __attribute__((ext_vector_type(4)));
typedef __bf16 bf16x2 __attribute__((ext_vector_type(2)));
typedef __bf16 bf16x8 __attribute__((ext_vector_type(8)));
typedef _Float16 f16x2 __attribute__((ext_vector_type(2)));
typedef _Float16 f16x8 __attribute__((ext_vector_type(8)));
#define LDS __attribute__((address_space(3)))
#define GLB __attribute__((address_space(1)))
#define DEV __device__ __forceinline__

constexpr u32 WS_GEN = 0;
constexpr u32 WS_FS = 4096;
constexpr u32 WS_FH = WS_FS + 32 * 128;
constexpr u32 WS_FY = WS_FH + 2048 * 128;
constexpr u32 WS_MEAN = WS_FY + 1024 * 128;
constexpr u32 WS_RSTD = WS_MEAN + 8192;
constexpr u32 WS_STAMP = WS_RSTD + 8192;
constexpr u32 WS_U = 1u << 20;
constexpr u32 WS_Y = 3u << 20;
constexpr u32 WS_H = 5u << 20;
constexpr u32 WS_END = WS_H + 2048 * 1792 * 2;

constexpr u32 SLOT = 49152;
constexpr u32 L_SP = 3 * SLOT;
constexpr u32 L_KX = L_SP;
constexpr u32 L_POLL = L_SP;
constexpr u32 L_CS = L_SP + 1024;
constexpr u32 L_MU = L_CS + 512;
constexpr u32 L_CAR = L_MU + 1024;
constexpr u32 L_MISC = L_SP + 5120;
constexpr u32 L_DUMMY = L_MISC + 256;
constexpr u32 L_KA = L_SP + 5632;
constexpr u32 L_U = 2 * SLOT + 32768;
constexpr u32 L_PA01 = SLOT + 32768;
constexpr u32 L_PA23 = 2 * SLOT + 32768;
static_assert(L_DUMMY + 256 == L_KA && L_KA + 10752 == 163840 && L_CAR + 1056 <= L_MISC, "lds map");

constexpr u64 TAG_STEP = 0x9E3779B97F4A7C15ull;
constexpr int CONV_EARLY = 8, CONV_MID = 44;
constexpr u32 CODE_BYTES = 35840;
#define VMCNT(n) asm volatile("s_waitcnt vmcnt(%0)" ::"n"(n) : "memory")
#define LGKM0() asm volatile("s_waitcnt lgkmcnt(0)" ::: "memory")
#define BAR() __builtin_amdgcn_s_barrier()
#define SCHED() __builtin_amdgcn_sched_barrier(0)

#define STAMP(i)
#define STAMP_OUT()

DEV void dma16x4(const void* sb, u32 lds, u32 v0, u32 v1, u32 v2, u32 v3) {
  asm volatile("s_mov_b32 m0, %1\n\ts_nop 0\n\t"
               "global_load_lds_dwordx4 %2, %0\n\t"
               "global_load_lds_dwordx4 %3, %0 offset:1024\n\t"
               "global_load_lds_dwordx4 %4, %0 offset:2048\n\t"
               "global_load_lds_dwordx4 %5, %0 offset:3072" ::"s"(sb), "s"(lds), "v"(v0), "v"(v1), "v"(v2), "v"(v3) : "memory");
}
DEV void dma16x2_sc1(const void* sb, u32 lds, u32 v0) {
  asm volatile("s_mov_b32 m0, %1\n\ts_nop 0\n\t"
               "global_load_lds_dwordx4 %2, %0 sc1\n\t"
               "global_load_lds_dwordx4 %2, %0 offset:1024 sc1" ::"s"(sb), "s"(lds), "v"(v0) : "memory");
}
DEV void dma4(const void* sb, u32 lds, u32 v0) {
  asm volatile("s_mov_b32 m0, %1\n\ts_nop 0\n\tglobal_load_lds_dword %2, %0" ::"s"(sb), "s"(lds), "v"(v0) : "memory");
}
DEV void dma4_sc1(const void* sb, u32 lds, u32 v0) {
  asm volatile("s_mov_b32 m0, %1\n\ts_nop 0\n\tglobal_load_lds_dword %2, %0 sc1" ::"s"(sb), "s"(lds), "v"(v0) : "memory");
}
DEV void st16_sc1(const void* sb, u32 voff, u32x4 v) {
  asm volatile("global_store_dwordx4 %0, %1, %2 sc1\n\ts_nop 1" ::"v"(voff), "v"(v), "s"(sb) : "memory");
}
DEV float bflo(u32 v) { return __builtin_bit_cast(float, v << 16); }
DEV float bfhi(u32 v) { return __builtin_bit_cast(float, v & 0xffff0000u); }
DEV u32 pkbf(float a, float b) { f32x2 v = {a, b}; return __builtin_bit_cast(u32, __builtin_convertvector(v, bf16x2)); }
DEV u32 pkh(float a, float b) { f32x2 v = {a, b}; return __builtin_bit_cast(u32, __builtin_convertvector(v, f16x2)); }
DEV float rbf(float a) { return bflo(pkbf(a, a)); }
template <int CTRL> DEV float dppf(float v) { return __builtin_bit_cast(float, __builtin_amdgcn_update_dpp(0, __builtin_bit_cast(int, v), CTRL, 0xf, 0xf, true)); }
template <int CTRL> DEV u32 dppu(u32 v) { return (u32)__builtin_amdgcn_update_dpp(0, (int)v, CTRL, 0xf, 0xf, true); }
DEV f32x2 cmul(f32x2 a, f32x2 b) {
  f32x2 t = f32x2{a.x, a.x} * b;
  return __builtin_elementwise_fma(f32x2{a.y, a.y}, f32x2{-b.y, b.x}, t);
}
DEV u32x4 trpair(LDS char* p0, LDS char* p1) {
  u32x2 a = __builtin_bit_cast(u32x2, __builtin_amdgcn_ds_read_tr16_b64_v4i16((LDS s16x4*)p0));
  u32x2 c = __builtin_bit_cast(u32x2, __builtin_amdgcn_ds_read_tr16_b64_v4i16((LDS s16x4*)p1));
  return u32x4{a.x, a.y, c.x, c.y};
}
DEV f32x16 mma_bf(u32x4 a, u32x4 b, f32x16 c) {
  return __builtin_amdgcn_mfma_f32_32x32x16_bf16(__builtin_bit_cast(bf16x8, a), __builtin_bit_cast(bf16x8, b), c, 0, 0, 0);
}
DEV f32x16 mma_h(u32x4 a, u32x4 b, f32x16 c) {
  return __builtin_amdgcn_mfma_f32_32x32x16_f16(__builtin_bit_cast(f16x8, a), __builtin_bit_cast(f16x8, b), c, 0, 0, 0);
}
DEV void set_flag(char* ws, u32 off, u64 tag) {
  __hip_atomic_store((GLB u64*)(ws + off), tag, __ATOMIC_RELAXED, __HIP_MEMORY_SCOPE_AGENT);
}
DEV void wait_flags(char* ws, u32 off, bool act, u64 tag) {
#pragma unroll 1
  for (int spins = 0; spins < (1 << 18); ++spins) {
    u64 v = tag;
    if (act) v = __hip_atomic_load((GLB u64*)(ws + off), __ATOMIC_RELAXED, __HIP_MEMORY_SCOPE_AGENT);
    if (__all(v == tag)) break;
    __builtin_amdgcn_s_sleep(2);
  }
  asm volatile("" ::: "memory");
}
DEV void wait_lds_cnt(LDS char* sm, u32 off, u32 want) {
#pragma unroll 1
  for (int spins = 0; spins < (1 << 20); ++spins) {
    u32 v = __hip_atomic_load((LDS u32*)(sm + off), __ATOMIC_RELAXED, __HIP_MEMORY_SCOPE_WORKGROUP);
    if (__builtin_amdgcn_readfirstlane(v) >= want) break;
  }
  asm volatile("" ::: "memory");
}
DEV void lds_inc(LDS char* sm, u32 off, int lane) {
  if (lane == 0) asm volatile("ds_add_u32 %0, %1" ::"v"((u32)(size_t)sm + off), "v"(1u) : "memory");
}

template <int PS> struct ConvPlan {
  int k[72], t[72], first[72];
  constexpr ConvPlan() : k{}, t{}, first{} {
    const int T[4] = {PS ? 1 : 0, PS ? 2 : 3, PS ? 5 : 4, PS ? 6 : 7};
    int n = 0;
    for (int kk = 0; kk < 32; ++kk) {
      bool f = true;
      for (int i = 0; i < 4; ++i)
        if (kk < 4 * T[i] + 4) { k[n] = kk; t[n] = i; first[n] = f; f = false; ++n; }
    }
  }
};
template <int PS, class Early, class Mid>
DEV void conv_mm(LDS char* kp, LDS char* up, f32x16 (&acc)[4], Early early, Mid mid) {
  constexpr int T[4] = {PS ? 1 : 0, PS ? 2 : 3, PS ? 5 : 4, PS ? 6 : 7};
  constexpr ConvPlan<PS> P;
  constexpr int D = 4;
  u32x4 kf[32], uf[72];
  f32x16 zero;
#pragma unroll
  for (int e = 0; e < 16; ++e) zero[e] = 0.f;
  auto fetch = [&](int n) {
    if (P.first[n]) kf[P.k[n]] = trpair(kp + 128 * P.k[n], kp + 128 * P.k[n] - 32);
    uf[n] = *(LDS u32x4*)(up + 512 * T[P.t[n]] - 128 * P.k[n]);
  };
#pragma unroll
  for (int n = 0; n < D; ++n) fetch(n);
  SCHED();
#pragma unroll
  for (int n = 0; n < 72; ++n) {
    acc[P.t[n]] = mma_h(uf[n], kf[P.k[n]], P.k[n] == 0 ? zero : acc[P.t[n]]);
    SCHED();
    if (n + D < 72) fetch(n + D);
    if (n == CONV_EARLY) early();
    if (n == CONV_MID) mid();
    SCHED();
  }
}

__global__ __launch_bounds__(512, 2) void fused_kernel(const u16* __restrict__ x, const u16* __restrict__ lar, const u16* __restrict__ aim,
                                                       const u16* __restrict__ cre, const u16* __restrict__ cim, const u16* __restrict__ ldt,
                                                       const u16* __restrict__ dsk, const u16* __restrict__ wg, const u16* __restrict__ wu,
                                                       const u16* __restrict__ wd, u16* __restrict__ out, char* ws) {
  __shared__ __attribute__((aligned(16))) char smem[163840];
  LDS char* sm = (LDS char*)smem;
  const u32 sb = (u32)(size_t)sm;
  {
    u64 pc0; asm volatile("s_getpc_b64 %0" : "=s"(pc0));
    dma4((const char*)(pc0 & ~127ull), sb + L_DUMMY, min(((blockIdx.x >> 3) << 10) + ((threadIdx.x >> 6) << 7) + ((threadIdx.x & 1) << 15), CODE_BYTES - 128u));
  }
  asm volatile("" ::"s"(x), "s"(lar), "s"(aim), "s"(cre), "s"(cim), "s"(ldt), "s"(dsk), "s"(wg), "s"(wu), "s"(wd), "s"(out), "s"(ws));
  const int b = blockIdx.x, xcd = b & 7, idx = b >> 3;
  const int lane = threadIdx.x & 63;
  const int w = __builtin_amdgcn_readfirstlane(threadIdx.x >> 6), q = w & 3;
  const int a2 = xcd & 1, bq = xcd >> 1;
  const int mt = a2 * 4 + (idx & 3), ft = bq * 7 + (idx >> 2);
  const int s0 = (2 * bq + 4 * a2) & 7;
  const bool helper = idx >= 28;
  const int hb = a2 * 16 + bq * 4 + (idx & 3);
  const int rt = bq * 8 + (idx & 7), ct = a2 * 4 + (idx >> 3);
  const int B128 = rt >> 1, sh = rt & 1;
  const int h = lane >> 5;
  const int chl = q >> 1, ch = 2 * b + chl;
  const auto wsr = __builtin_amdgcn_make_buffer_rsrc(ws, 0, WS_END, 0x00020000);

  u32 l_lar, l_aim, l_cr, l_ci, l_ldt;
  u64 l_gen;
  asm volatile("global_load_ushort %0, %6, %8\n\tglobal_load_ushort %1, %6, %9\n\tglobal_load_ushort %2, %6, %10\n\t"
               "global_load_ushort %3, %6, %11\n\tglobal_load_ushort %4, %7, %12\n\tglobal_load_dwordx2 %5, %7, %13 sc1"
               : "=&v"(l_lar), "=&v"(l_aim), "=&v"(l_cr), "=&v"(l_ci), "=&v"(l_ldt), "=&v"(l_gen)
               : "v"((b * 256 + (threadIdx.x & 255)) * 2), "v"(0u), "s"(lar), "s"(aim), "s"(cre), "s"(cim), "s"(ldt + ch), "s"(ws + WS_GEN) : "memory");
  const char* xb = (const char*)x + (size_t)mt * 256 * 1024;
  const char* xtouch = xb + (idx >> 3) * 65536;
  if (w < 3) {
    const char* tb = q == 0 ? xtouch + (s0 + ((idx >> 2) & 1)) * 128
                            : (const char*)(q == 1 ? wg : wu) + (helper ? 0 : (((s0 + (idx & 3)) & 7) * 64) * 3584 + ft * 128);
    dma4(tb, sb + L_DUMMY, lane * (q == 0 ? 1024 : helper ? 0 : 3584));
  }
  const u32 cp = lane & 7, l3 = lane >> 3, l4 = lane >> 4;
  u32 vx[4], vw[4];
#pragma unroll
  for (int j = 0; j < 4; ++j) {
    vx[j] = (64 * q + 8 * j + l3) * 1024 + ((cp ^ ((4 * j + l4) & 7)) << 4) - 1024 * j;
    vw[j] = (32 * (q & 1) + 8 * j + l3) * 3584 + ((cp ^ (((l4 & 1) | (q & 2)) << 1)) << 4) - 1024 * j;
  }
  const char* wb = (const char*)((q & 2) ? wu : wg) + ft * 128;
  auto stage = [&](int k, u32 slot) {
    const int s = (k + s0) & 7;
    dma16x4(xb + s * 128, slot + q * 12288, vx[0], vx[1], vx[2], vx[3]);
    dma16x4(wb + s * (64 * 3584), slot + q * 12288 + 8192, vw[0], vw[1], vw[2], vw[3]);
    dma16x4(xb + s * 128 + 32768, slot + q * 12288 + 4096, vx[0], vx[1], vx[2], vx[3]);
  };
  if (lane < 8) *(LDS u32*)(sm + L_MISC + 48 + lane * 4) = 0u;
  LGKM0();
  BAR();
  if (w < 4 && !helper) stage(0, sb);
  SCHED();
  auto wtouch = [&](int i0, int i1) {
#pragma unroll 1
    for (int i = i0; i < i1; ++i) {
      const int c = (i * 4 + q + bq * 7) % 28;
      dma4((const char*)wd + (size_t)c * 65536 + a2 * 512 + (idx & 3) * 128, sb + L_DUMMY, lane * 1024);
    }
  };

  if (helper) {
    u32x4 xr[8];
#pragma unroll
    for (int j = 0; j < 8; ++j) xr[j] = *(const u32x4*)((const char*)x + (size_t)(hb * 64 + 32 * (w >> 2) + 4 * j + q) * 1024 + lane * 16);
    u64 tag;
    asm volatile("s_waitcnt vmcnt(8)\n\tv_mov_b64 %0, %1" : "=&v"(tag) : "v"(l_gen) : "memory");
    tag += TAG_STEP;
    float d[8][8], s[8];
#pragma unroll
    for (int j = 0; j < 8; ++j) {
#pragma unroll
      for (int e = 0; e < 4; ++e) { d[j][2 * e] = bflo(xr[j][e]); d[j][2 * e + 1] = bfhi(xr[j][e]); }
      const f32x2 t = (f32x2{d[j][0], d[j][1]} + f32x2{d[j][2], d[j][3]}) + (f32x2{d[j][4], d[j][5]} + f32x2{d[j][6], d[j][7]});
      s[j] = t.x + t.y;
    }
    asm volatile("" : "+v"(s[7]));
    STAMP(G_Hx);
    if (w < 4) wtouch(0, 1);
    auto red8 = [&](float (&v)[8], float (&u)[2]) {
      float t[4];
#pragma unroll
      for (int i = 0; i < 4; ++i) {
        auto r = __builtin_amdgcn_permlane32_swap(__builtin_bit_cast(u32, v[i]), __builtin_bit_cast(u32, v[i + 4]), false, false);
        t[i] = __builtin_bit_cast(float, (u32)r[0]) + __builtin_bit_cast(float, (u32)r[1]);
      }
#pragma unroll
      for (int i = 0; i < 2; ++i) {
        auto r = __builtin_amdgcn_permlane16_swap(__builtin_bit_cast(u32, t[i]), __builtin_bit_cast(u32, t[i + 2]), false, false);
        float z = __builtin_bit_cast(float, (u32)r[0]) + __builtin_bit_cast(float, (u32)r[1]);
        z += dppf<0x128>(z); z += dppf<0x124>(z); z += dppf<0x122>(z); z += dppf<0x121>(z);
        u[i] = z;
      }
    };
    auto bcast = [&](float (&u)[2], int j) { return __builtin_bit_cast(float, __builtin_amdgcn_readlane(__builtin_bit_cast(int, u[j & 1]), 16 * (j >> 1))); };
    float mean[2], var[2], rs[2], of[2];
    red8(s, mean);
    mean[0] *= (1.f / 512.f); mean[1] *= (1.f / 512.f);
#pragma unroll
    for (int j = 0; j < 8; ++j) {
      const float m = bcast(mean, j);
      f32x2 a2v = {0.f, 0.f};
#pragma unroll
      for (int e = 0; e < 4; ++e) {
        const f32x2 t = f32x2{d[j][2 * e], d[j][2 * e + 1]} - f32x2{m, m};
        d[j][2 * e] = t.x; d[j][2 * e + 1] = t.y;
        a2v = __builtin_elementwise_fma(t, t, a2v);
      }
      s[j] = a2v.x + a2v.y;
    }
    red8(s, var);
#pragma unroll
    for (int i = 0; i < 2; ++i) {
      const float mb = rbf(mean[i]);
      rs[i] = __builtin_amdgcn_rcpf(rbf(__builtin_amdgcn_sqrtf(rbf(rbf(var[i] * (1.f / 512.f)) + 1e-5f))));
      of[i] = (mean[i] - mb) * rs[i];
      mean[i] = mb;
    }
    if ((lane & 15) == 0) {
#pragma unroll
      for (int i = 0; i < 2; ++i) {
        const int row = 32 * (w >> 2) + 4 * (i + 2 * (lane >> 4)) + q;
        *(LDS float*)(sm + 65536 + row * 4) = mean[i];
        *(LDS float*)(sm + 65536 + 256 + row * 4) = rs[i];
      }
    }
#pragma unroll
    for (int j = 0; j < 8; ++j) {
      const float r = bcast(rs, j), o = bcast(of, j);
#pragma unroll
      for (int e = 0; e < 4; ++e) {
        const f32x2 t = __builtin_elementwise_fma(f32x2{d[j][2 * e], d[j][2 * e + 1]}, f32x2{r, r}, f32x2{o, o});
        d[j][2 * e] = t.x; d[j][2 * e + 1] = t.y;
      }
    }
#pragma unroll
    for (int i = 0; i < 8; ++i) {
      const u32x4 o = {pkh(d[0][i], d[1][i]), pkh(d[2][i], d[3][i]), pkh(d[4][i], d[5][i]), pkh(d[6][i], d[7][i])};
      *(LDS u32x4*)(sm + (8 * lane + i) * 128 + ((w ^ (lane & 7)) << 4)) = o;
    }
    LGKM0();
    STAMP(G_Hln);
    BAR();
    if (w == 0) {
      __builtin_amdgcn_raw_buffer_store_b32(*(LDS u32*)(sm + 65536 + lane * 4), wsr, lane * 4, WS_MEAN + hb * 256, 16);
      __builtin_amdgcn_raw_buffer_store_b32(*(LDS u32*)(sm + 65536 + 256 + lane * 4), wsr, lane * 4, WS_RSTD + hb * 256, 16);
    }
    u32x4 v[8];
#pragma unroll
    for (int it = 0; it < 8; ++it) v[it] = *(LDS u32x4*)(sm + (64 * w + 8 * it + (lane >> 3)) * 128 + (((lane & 7) ^ it) << 4));
#pragma unroll
    for (int it = 0; it < 8; ++it) st16_sc1(ws + WS_U + (64 * w + 8 * it) * 4096 + hb * 128, (lane >> 3) * 4096 + (lane & 7) * 16, v[it]);
    STAMP(G_Hst);
    VMCNT(0);
    BAR();
    if (threadIdx.x == 0) set_flag(ws, WS_FS + hb * 128, tag);
    STAMP(G_Hflag);
  }
  SCHED();

  if (w < 4) {
    if (!helper) {
      const int mh = w >> 1, fp = w & 1;
      const u32 r31 = lane & 31, qq = (lane & 15) >> 2, gu = (lane >> 4) & 1, p4 = lane & 3;
      LDS char* xa[3][4];
      LDS char* wa[3][2];
#pragma unroll
      for (int s = 0; s < 3; ++s) {
#pragma unroll
        for (int ks = 0; ks < 4; ++ks) {
          xa[s][ks] = sm + s * SLOT + mh * 24576 + r31 * 128 + (((2 * ks + h) ^ ((r31 >> 1) & 7)) << 4);
          asm volatile("" : "+v"(xa[s][ks]));
        }
#pragma unroll
        for (int j = 0; j < 2; ++j) {
          wa[s][j] = sm + s * SLOT + 8192 + gu * 24576 + (8 * h + qq) * 128 + (((2 * fp + (j ^ mh)) ^ (((qq >> 1) & 1) | (gu << 1))) << 5) + (((p4 & 1) << 1) | (p4 >> 1)) * 8;
          asm volatile("" : "+v"(wa[s][j]));
        }
      }
      const u32 pq = (lane >> 2) & 3;
      LDS char* csw = sm + L_CS + (2 * fp + mh) * 128 + gu * 64 + (((pq & 1) << 1) | (pq >> 1)) * 16 + p4 * 4;
      LDS char* csr[2] = {sm + L_CS + (2 * fp + mh) * 128 + h * 32, sm + L_CS + (2 * fp + (mh ^ 1)) * 128 + h * 32};
      LDS char* mur = sm + L_MU + (mh * 128 + r31) * 4;
      const char* hb0 = ws + WS_H + (((mt * 8 + mh * 4) * 28 + ft) * 4 + 2 * fp) * 1024;
      u32 hv[2] = {(u32)(lane * 16 + mh * 1024), (u32)(lane * 16 + (mh ^ 1) * 1024)};
      asm volatile("" : "+v"(csw), "+v"(csr[0]), "+v"(csr[1]), "+v"(mur), "+v"(hv[0]), "+v"(hv[1]));
      stage(1, sb + SLOT);
      STAMP(G_issued);
      f32x16 acc[4][2];
      f32x16 zero;
#pragma unroll
      for (int e = 0; e < 16; ++e) zero[e] = 0.f;
      u32x4 xf[2][4];
      u32x2 wf[2][2][2];
      float cs = 0.f;
      auto rd = [&](int buf, int g, int ks, int sl, int i0) {
        if (g < 4) wf[buf][g >> 1][g & 1] = __builtin_bit_cast(u32x2, __builtin_amdgcn_ds_read_tr16_b64_v4i16((LDS s16x4*)(wa[sl][g >> 1] + (ks >> 1) * 12288 + (ks & 1) * 2048 + (g & 1) * 512)));
        else { const int i = i0 + g - 4; xf[buf][g - 4] = *(LDS u32x4*)(xa[sl][ks] + (i >> 1) * 12288 + (i & 1) * 4096); }
      };
      auto csum = [&](int buf, int e) { const u32 v = wf[buf][0][e >> 2][(e >> 1) & 1]; cs += (e & 1) ? bfhi(v) : bflo(v); };
      auto wfrag = [&](int c, int j) { return u32x4{wf[c][j][0].x, wf[c][j][0].y, wf[c][j][1].x, wf[c][j][1].y}; };
      asm volatile("s_waitcnt vmcnt(12)" ::"v"(l_lar), "v"(l_aim), "v"(l_cr), "v"(l_ci), "v"(l_ldt), "v"(l_gen) : "memory");
      lds_inc(sm, L_MISC + 56, lane);
      if (w == 3) dma4(xtouch + ((s0 + 2 + ((idx >> 2) & 1)) & 7) * 128, sb + L_DUMMY, lane * 1024);
      wait_lds_cnt(sm, L_MISC + 56, 4);
      STAMP(G_st0);
#pragma unroll
      for (int g = 0; g < 8; ++g) rd(0, g, 0, 0, 0);
      SCHED();
#pragma unroll
      for (int st = 0; st < 24; ++st) {
        const int c = st & 1, n = c ^ 1, k = st >> 2, ks = st & 3;
        const bool last = ks == 3;
        const int nsl = ((st + 1) >> 2) % 3, nks = (st + 1) & 3, nr = st == 23 ? 6 : 8;
#pragma unroll
        for (int g = 0; g < 8; ++g) {
          if (last && g == 2) {
            if (k == 0) { if (w == 3) VMCNT(1); else VMCNT(0); }
            __builtin_amdgcn_s_waitcnt(0xC07F); BAR();
            if (k == 0) STAMP(G_B1);
            if (k == 1) STAMP(G_B2);
            if (k == 3) STAMP(G_B4);
            if (k == 5) STAMP(G_B6);
            SCHED();
          }
          acc[g >> 1][g & 1] = mma_bf(wfrag(c, g & 1), xf[c][g >> 1], st == 0 ? zero : acc[g >> 1][g & 1]);
          SCHED();
          if (!last) rd(n, g, nks, nsl, 0);
          else if (g >= 2) {
            if (g < 4) { rd(n, 2 * g - 4, nks, nsl, 0); rd(n, 2 * g - 3, nks, nsl, 0); }
            else if (g < nr) rd(n, g, nks, nsl, 0);
          }
          csum(c, g);
          SCHED();
        }
      }
      float mu[4];
      f32x4 cg[2][2], cu[2][2];
      u32x4 ho;
      auto epi = [&](int i, int j, int e) {
        const float g0 = fmaf(cg[j][e >> 1][(2 * e) & 3], mu[i], acc[i][j][2 * e]), g1 = fmaf(cg[j][e >> 1][(2 * e + 1) & 3], mu[i], acc[i][j][2 * e + 1]);
        const float u0 = fmaf(cu[j][e >> 1][(2 * e) & 3], mu[i], acc[i][j][8 + 2 * e]), u1 = fmaf(cu[j][e >> 1][(2 * e + 1) & 3], mu[i], acc[i][j][9 + 2 * e]);
        ho[e] = pkbf(g0 * u0, g1 * u1);
        if (e == 3) st16_sc1(hb0 + i * (28 * 4096), hv[j], ho);
      };
#pragma unroll
      for (int st = 0; st < 16; ++st) {
        const int c = st & 1, n = c ^ 1, pr = st >> 3;
        const int nst = st + 1, nsl = (nst >> 2) & 1, nks = nst & 3, npr = nst >> 3;
        const bool bar = st == 3 || st == 9 || st == 11 || st == 15;
#pragma unroll
        for (int g = 0; g < 4; ++g) {
          if (bar && g == 1) {
            __builtin_amdgcn_s_waitcnt(0xC07F); BAR();
            STAMP(st == 3 ? G_B7 : st == 9 ? G_B8 : st == 11 ? G_B9 : G_B10);
            SCHED();
          }
          const int i = 2 * pr + (g >> 1), j = g & 1;
          acc[i][j] = mma_bf(wfrag(c, j), xf[c][g >> 1], acc[i][j]);
          SCHED();
          const int gg = bar ? g - 1 : g;
          if (st < 15 && gg >= 0 && gg < 3) {
            constexpr int ord[6] = {0, 1, 4, 2, 3, 5};
            rd(n, ord[2 * gg], nks, nsl, 2 * npr); rd(n, ord[2 * gg + 1], nks, nsl, 2 * npr);
          }
          if (st < 8) { csum(c, 2 * g); csum(c, 2 * g + 1); }
          if (st == 8 && g == 0) {
            auto r = __builtin_amdgcn_permlane32_swap(__builtin_bit_cast(u32, cs), __builtin_bit_cast(u32, cs), false, false);
            *(LDS float*)csw = __builtin_bit_cast(float, (u32)r[0]) + __builtin_bit_cast(float, (u32)r[1]);
          }
          if (st == 8 && (g == 1 || g == 2)) {
            mu[2 * g - 2] = -*(LDS float*)(mur + (2 * g - 2) * 128);
            mu[2 * g - 1] = -*(LDS float*)(mur + (2 * g - 1) * 128);
          }
          const int t = st * 4 + g - 37;
          if (t >= 0 && t < 4) {
            cg[t >> 1][t & 1] = *(LDS f32x4*)(csr[t >> 1] + (t & 1) * 16);
            cu[t >> 1][t & 1] = *(LDS f32x4*)(csr[t >> 1] + 64 + (t & 1) * 16);
          }
          if (t >= 4) {
            const int v = t - 4;
            if (v % 3 != 2 && v - v / 3 < 16) { const int pc = v - v / 3; epi(pc >> 3, (pc >> 2) & 1, pc & 3); }
          }
          SCHED();
        }
      }
#pragma unroll
      for (int i = 2; i < 4; ++i)
#pragma unroll
        for (int j = 0; j < 2; ++j) {
          u32x4 o;
#pragma unroll
          for (int e = 0; e < 4; ++e) {
            const f32x2 m2 = {mu[i], mu[i]};
            const f32x2 g2 = __builtin_elementwise_fma(f32x2{cg[j][e >> 1][(2 * e) & 3], cg[j][e >> 1][(2 * e + 1) & 3]}, m2, f32x2{acc[i][j][2 * e], acc[i][j][2 * e + 1]});
            const f32x2 u2 = __builtin_elementwise_fma(f32x2{cu[j][e >> 1][(2 * e) & 3], cu[j][e >> 1][(2 * e + 1) & 3]}, m2, f32x2{acc[i][j][8 + 2 * e], acc[i][j][9 + 2 * e]});
            const f32x2 h2 = g2 * u2;
            o[e] = pkbf(h2.x, h2.y);
          }
          st16_sc1(hb0 + i * (28 * 4096), hv[j], o);
        }
      STAMP(G_hstore);
    } else {
      wtouch(1, 7);
      STAMP(G_Htouch);
#pragma unroll
      for (int i = 0; i < 10; ++i) BAR();
    }

    const u64 tag = *(LDS u64*)(sm + L_MISC);
    const float ysc = 2.f * *(LDS float*)(sm + L_MISC + 40 + chl * 4) *
                      fmaxf(*(LDS float*)(sm + L_MISC + 8 + chl * 8), *(LDS float*)(sm + L_MISC + 12 + chl * 8));
    const int t = threadIdx.x, sr = t >> 3, dg = t & 7;
    const int rowA = 128 * B128 + 32 * sh + sr;
    u32x4 xA, xB, dv;
    float mm[2], rr[2];
    {
      f32x16 z[4];
      const int ps = w & 1;
      LDS char* kp = sm + L_KA + chl * 4160 + 64 + (32 * h + 16 * ((lane >> 4) & 1) + 4 * (lane & 3) - 4 * ((lane & 15) >> 2)) * 2;
      LDS char* up = sm + L_U + chl * 4608 + 512 + (lane & 31) * 16 - 64 * h;
      const u32 fh = WS_FH + ((mt * 4 + w) * 32 + ft) * 128;
      auto early = [&]() {
        VMCNT(4);
        if (lane == 0 && !helper) set_flag(ws, fh, tag);
        STAMP(G_hflagA);
      };
      auto mid = [&]() {
        VMCNT(0);
        if (lane == 0 && !helper) set_flag(ws, fh + 1024 * 128, tag);
        STAMP(G_hflagB);
        xA = *(const u32x4*)((const char*)x + (size_t)rowA * 1024 + ct * 128 + dg * 16);
        xB = *(const u32x4*)((const char*)x + (size_t)(rowA + 64) * 1024 + ct * 128 + dg * 16);
        dv = *(const u32x4*)((const char*)dsk + ct * 128 + dg * 16);
#pragma unroll
        for (int m = 0; m < 2; ++m) {
          mm[m] = __builtin_bit_cast(float, __builtin_amdgcn_raw_buffer_load_b32(wsr, (rowA + 64 * m) * 4, WS_MEAN, 16));
          rr[m] = __builtin_bit_cast(float, __builtin_amdgcn_raw_buffer_load_b32(wsr, (rowA + 64 * m) * 4, WS_RSTD, 16));
        }
      };
      if (ps) conv_mm<1>(kp, up, z, early, mid); else conv_mm<0>(kp, up, z, early, mid);
      STAMP(G_convmm);
      int T[4];
      T[0] = ps; T[1] = 3 - ps; T[2] = 4 + ps; T[3] = 7 - ps;
      LDS char* car = sm + L_CAR + chl * 528;
#pragma unroll
      for (int i = 0; i < 4; ++i)
#pragma unroll
        for (int g = 0; g < 4; ++g) {
          z[i][4 * g + 2] += dppf<0x138>(z[i][4 * g + 3]);
          z[i][4 * g + 1] += dppf<0x138>(z[i][4 * g + 2]);
        }
      if (lane == 63) {
#pragma unroll
        for (int i = 0; i < 4; ++i)
#pragma unroll
          for (int g = 0; g < 4; ++g)
            *(LDS f32x4*)(car + (4 * T[i] + g + 1) * 16) = f32x4{z[i][4 * g], z[i][4 * g + 1], z[i][4 * g + 2], z[i][4 * g + 3]};
      }
      SCHED();
#pragma unroll
      for (int i = 0; i < 4; ++i)
#pragma unroll
        for (int g = 0; g < 4; ++g) z[i][4 * g] += dppf<0x138>(z[i][4 * g + 1]);
      LGKM0();
      lds_inc(sm, L_MISC + 48 + chl * 4, lane);
      wait_lds_cnt(sm, L_MISC + 48 + chl * 4, 2);
      STAMP(G_carry);
      if (lane < 3) {
#pragma unroll
        for (int i = 0; i < 4; ++i)
#pragma unroll
          for (int g = 0; g < 4; ++g) z[i][4 * g] += *(LDS float*)(car + (4 * T[i] + g) * 16 + 4 + lane * 4);
      }
#pragma unroll
      for (int i = 0; i < 4; ++i)
#pragma unroll
        for (int g = 0; g < 2; ++g)
          __builtin_amdgcn_raw_buffer_store_b32(pkbf(z[i][8 * g] * ysc, z[i][8 * g + 4] * ysc), wsr, h * 65536 + (lane & 31) * 4,
                                                WS_Y + (2 * T[i] + g) * 131072 + ch * 128, 16);
    }
    STAMP(G_ystore);
    VMCNT(0);
    if (lane == 0) set_flag(ws, WS_FY + ((w & 1) * 512 + ch) * 128, tag);
    STAMP(G_yflag);
    float base[2][8], r2[2];
#pragma unroll
      for (int m = 0; m < 2; ++m) {
        r2[m] = rr[m] * rr[m];
#pragma unroll
        for (int e = 0; e < 4; ++e) {
          const u32 xv = m ? xB[e] : xA[e];
          base[m][2 * e] = fmaf((bflo(xv) - mm[m]) * rr[m], bflo(dv[e]), bflo(xv));
          base[m][2 * e + 1] = fmaf((bfhi(xv) - mm[m]) * rr[m], bfhi(dv[e]), bfhi(xv));
        }
      }
#pragma unroll
    for (int e = 0; e < 8; ++e) asm volatile("" : "+v"(base[0][e]), "+v"(base[1][e]));
    {
      const int cti = B128 >> 1, psn = (cti ^ (cti >> 1)) & 1;
      wait_flags(ws, WS_FY + (psn * 512 + ct * 64 + 16 * w + (lane & 15)) * 128, true, tag);
    }
    STAMP(G_ypoll);
    {
      const u32 yo = WS_Y + B128 * 131072 + sh * 65536 + (ct * 64 + 16 * w) * 128;
      const u32x4 y0 = __builtin_amdgcn_raw_buffer_load_b128(wsr, lane * 16, yo, 16);
      const u32x4 y1 = __builtin_amdgcn_raw_buffer_load_b128(wsr, lane * 16, yo + 1024, 16);
      LDS char* yt = sm + L_KA + chl * 4160;
      const u32 cl = (w & 1) * 16 + (lane >> 3), sq = lane & 7;
#pragma unroll
      for (int e = 0; e < 4; ++e) {
        *(LDS u32*)(yt + (4 * sq + e) * 128 + (((cl >> 2) ^ sq) << 4) + (cl & 3) * 4) = y0[e];
        *(LDS u32*)(yt + (4 * sq + e) * 128 + ((((cl + 8) >> 2) ^ sq) << 4) + (cl & 3) * 4) = y1[e];
      }
    }
    LGKM0();
    lds_inc(sm, L_MISC + 60, lane);
    STAMP(G_yld);
    u32x4 yv[2];
#pragma unroll
    for (int m = 0; m < 2; ++m) {
      if (m == 0) wait_lds_cnt(sm, L_MISC + 60, 8); else BAR();
      STAMP(m ? G_BB : G_A);
      if (m == 0) {
        LDS char* yt = sm + L_KA + (dg >> 2) * 4160 + sr * 128;
        yv[0] = *(LDS u32x4*)(yt + (((2 * (dg & 3)) ^ (sr >> 2)) << 4));
        yv[1] = *(LDS u32x4*)(yt + (((2 * (dg & 3) + 1) ^ (sr >> 2)) << 4));
      }
      f32x4 p[4][2];
#pragma unroll
      for (int k = 0; k < 4; ++k) {
        const u32 pb = m == 0 ? (k < 2 ? L_PA01 + k * 8192 : L_PA23 + (k - 2) * 8192) : 2 * SLOT + k * 8192;
#pragma unroll
        for (int e = 0; e < 2; ++e) p[k][e] = *(LDS f32x4*)(sm + pb + sr * 256 + (((2 * dg + e) ^ (sr & 7)) << 4));
      }
      u32x4 o;
#pragma unroll
      for (int e = 0; e < 4; ++e) {
        float v[2];
#pragma unroll
        for (int u = 0; u < 2; ++u) {
          const int c = 2 * e + u;
          const float s = (p[0][c >> 2][c & 3] + p[1][c >> 2][c & 3]) + (p[2][c >> 2][c & 3] + p[3][c >> 2][c & 3]);
          const u32 yw = yv[c >> 2][c & 3];
          v[u] = fmaf(s, r2[m], base[m][c] + (m ? bfhi(yw) : bflo(yw)));
        }
        o[e] = pkbf(v[0], v[1]);
      }
      *(u32x4*)((char*)out + (size_t)(rowA + 64 * m) * 1024 + ct * 128 + dg * 16) = o;
      if (m == 0) STAMP(G_Adone);
      if (m == 0 && b == 255 && w == 0) {
#pragma unroll 1
        for (int i = 0; i < 4; ++i) wait_flags(ws, WS_FY + (i * 128 + lane * 2) * 128, true, tag);
        if (lane == 0) set_flag(ws, WS_GEN, tag);
      }
    }
  } else {
    u32 p_lar, p_aim, p_cr, p_ci, p_ldt;
    u64 gen;
    asm volatile("s_waitcnt vmcnt(0)\n\tv_lshlrev_b32 %0, 16, %6\n\tv_lshlrev_b32 %1, 16, %7\n\tv_lshlrev_b32 %2, 16, %8\n\t"
                 "v_lshlrev_b32 %3, 16, %9\n\tv_lshlrev_b32 %4, 16, %10\n\tv_mov_b64 %5, %11"
                 : "=&v"(p_lar), "=&v"(p_aim), "=&v"(p_cr), "=&v"(p_ci), "=&v"(p_ldt), "=&v"(gen)
                 : "v"(l_lar), "v"(l_aim), "v"(l_cr), "v"(l_ci), "v"(l_ldt), "v"(l_gen) : "memory");
    const u64 tag = gen + TAG_STEP;
    __builtin_amdgcn_s_setprio(3);
    {
      float dt = rbf(__expf(__builtin_bit_cast(float, p_ldt)));
      STAMP(S_params);
      const float zr = -rbf(__expf(__builtin_bit_cast(float, p_lar))) * dt * 1.44269504f, zi = __builtin_bit_cast(float, p_aim) * dt * 0.15915494f;
      auto cexp = [&](float p, float sg) {
        const float mg = __builtin_amdgcn_exp2f(zr * p), t = __builtin_amdgcn_fractf(zi * p);
        return f32x2{mg * __builtin_amdgcn_cosf(t), sg * mg * __builtin_amdgcn_sinf(t)};
      };
      u32 mx = max(p_cr & 0x7fffffffu, p_ci & 0x7fffffffu);
      mx = max(mx, dppu<0x121>(mx)); mx = max(mx, dppu<0x122>(mx)); mx = max(mx, dppu<0x124>(mx)); mx = max(mx, dppu<0x128>(mx));
      u32 ex = max(max(__builtin_amdgcn_readlane(mx, 0), __builtin_amdgcn_readlane(mx, 16)), max(__builtin_amdgcn_readlane(mx, 32), __builtin_amdgcn_readlane(mx, 48)));
      ex = min(max(ex >> 23, 1u), 253u);
      const float isc = __builtin_bit_cast(float, (254u - ex) << 23);
      LDS char* sc = sm + 2 * SLOT + q * 12288;
      LDS char* sx = sm + L_KX + q * 1280 - 48 * 256;
      u32 fv[53];
      auto put = [&](int row, f32x2 v) { fv[row] = pkh(v.x, v.y); };
      const f32x2 w8 = cexp(8.f, 1.f), w64 = cexp(64.f, 1.f), w1 = cexp(1.f, -1.f), w256 = cexp(256.f, -1.f);
      f32x2 P[8], Q[4];
      P[0] = f32x2{1.f, 0.f}; P[1] = w8; P[2] = cmul(w8, w8); P[3] = cmul(P[2], w8); P[4] = cmul(P[2], P[2]);
      P[5] = cmul(P[1], P[4]); P[6] = cmul(P[2], P[4]); P[7] = cmul(P[3], P[4]);
      Q[1] = w64; Q[2] = cmul(w64, w64); Q[3] = cmul(Q[2], w64);
#pragma unroll
      for (int j = 0; j < 8; ++j) {
#pragma unroll
        for (int i = 0; i < 4; ++i) put(8 * i + j, i == 0 ? P[j] : j == 0 ? Q[i] : cmul(P[j], Q[i]));
      }
      f32x2 A[8];
      A[0] = f32x2{__builtin_bit_cast(float, p_cr) * isc, -__builtin_bit_cast(float, p_ci) * isc};
      const f32x2 w2 = cmul(w1, w1), w4 = cmul(w2, w2), w512 = cmul(w256, w256);
      A[1] = cmul(A[0], w1); A[2] = cmul(A[0], w2); A[3] = cmul(A[1], w2);
#pragma unroll
      for (int j = 0; j < 4; ++j) A[4 + j] = cmul(A[j], w4);
      const f32x2 E1 = cmul(A[0], w256), F1 = cmul(A[4], w256);
      const f32x2 E2 = cmul(A[0], w512), E3 = cmul(E1, w512);
      put(32, A[0]); put(40, E1);
      put(33, A[1]); put(41, cmul(A[2], w256));
      put(34, A[2]); put(42, F1);
      put(35, A[3]); put(43, cmul(A[6], w256));
      put(36, A[4]); put(44, E2);
      put(37, A[5]); put(45, cmul(A[4], w512));
      put(38, A[6]); put(46, E3);
      put(39, A[7]); put(47, cmul(F1, w512));
      const f32x2 E4 = cmul(E2, w512), E5 = cmul(E3, w512), E6 = cmul(E4, w512);
      put(48, E4); put(49, E5); put(50, E6); put(51, cmul(E5, w512)); put(52, cmul(E6, w512));
#pragma unroll
      for (int r = 0; r < 52; r += 4) asm volatile("" : "+v"(fv[r]), "+v"(fv[r + 1]), "+v"(fv[r + 2]), "+v"(fv[r + 3]));
      STAMP(S_valu);
#pragma unroll
      for (int r = 0; r < 53; ++r) {
        const int row = r < 48 ? (r >> 4) * 16 + ((r & 15) >> 1) + (r & 1) * 8 : r;
        *(LDS u32*)((row < 48 ? sc : sx) + row * 256 + ((lane * 4) ^ ((row & 7) << 4))) = fv[row];
      }
      f32x16 kk;
#pragma unroll
      for (int e = 0; e < 16; ++e) kk[e] = 0.f;
      const u32 ra = (lane & 31) * 256 + ((h ^ (lane & 7)) << 4);
      LDS char* la = ((lane & 16) ? sx : sc) + 8192;
      u32x4 fa[8], fb[8];
#pragma unroll
      for (int s = 0; s < 8; ++s) {
        const u32 o = (ra ^ ((s & 3) << 5)) + (s >> 2) * 128;
        fa[s] = *(LDS u32x4*)(la + o);
        fb[s] = *(LDS u32x4*)(sc + o);
      }
      LGKM0();
      SCHED();
      STAMP(S_factors);
      __builtin_amdgcn_s_setprio(0);
      if (!helper) stage(2, sb + 2 * SLOT);
      STAMP(S_st2);
      BAR();
      if (!helper) stage(3, sb);
      SCHED();
#pragma unroll
      for (int s = 0; s < 8; ++s) kk = mma_h(fa[s], fb[s], kk);
      LDS char* pp = sm + L_KA + q * 2688;
      const u32 c31 = lane & 31;
      *(LDS f32x4*)(pp + c31 * 32 + h * 16) = f32x4{kk[0], kk[1], kk[2], kk[3]};
      *(LDS f32x2*)(pp + (h ? 1536 + c31 * 8 : 1024 + c31 * 16)) = f32x2{kk[4], kk[5]};
      *(LDS f32x2*)(pp + (h ? 1792 + c31 * 8 : 1032 + c31 * 16)) = f32x2{kk[6], kk[7]};
      *(LDS float*)(pp + 2048 + h * 512 + c31 * 4) = kk[8];
      if (lane < 32) {
#pragma unroll
        for (int e = 9; e < 12; ++e) *(LDS float*)(pp + 2048 + (e - 8) * 128 + c31 * 4) = kk[e];
      }
      if (lane == 0) {
        *(LDS u32*)(sm + L_MISC + 8 + q * 4) = ex << 23;
        *(LDS float*)(sm + L_MISC + 40 + chl * 4) = dt;
        *(LDS u64*)(sm + L_MISC) = tag;
      }
      STAMP(S_kgen);
    }
    u32 vd[4];
#pragma unroll
    for (int j = 0; j < 4; ++j) vd[j] = (j >> 1) * 65536 + ((j & 1) * 8 + l3) * 1024 + ((cp ^ ((l4 & 1) << 2)) << 4) - 1024 * j;
    const char* db = (const char*)wd + q * 16384 + ct * 128;
    auto wd2 = [&](int j, u32 dst) { dma16x4(db + j * 65536, sb + dst, vd[0], vd[1], vd[2], vd[3]); };
    const u32 pollz = L_POLL + q * 256;
    const u32 tagw = h ? (u32)(tag >> 32) : (u32)tag;
    VMCNT(12); LGKM0();
    BAR();
    if (!helper) stage(4, sb + SLOT);
    u32 tp0, tp1; u32x2 tp2; u32x4 tp3;
    const u32 tt = (q & 1) * 64 + lane;
    {
      const float e0 = *(LDS float*)(sm + L_MISC + 8 + chl * 8), e1 = *(LDS float*)(sm + L_MISC + 12 + chl * 8);
      const float ie = __builtin_amdgcn_rcpf(fmaxf(e0, e1));
      const f32x2 f0 = {e0 * ie, e0 * ie}, f1 = {e1 * ie, e1 * ie};
      LDS char* pa = sm + L_KA + chl * 5376;
      u32 pk[4];
#pragma unroll
      for (int r = 0; r < 4; ++r) {
        const u32 o = r == 0 ? tt * 8 : 512 + r * 512 + tt * 4;
        const f32x2 a = {*(LDS float*)(pa + o), *(LDS float*)(pa + o + 4)};
        const f32x2 c = {*(LDS float*)(pa + 2688 + o), *(LDS float*)(pa + 2688 + o + 4)};
        const f32x2 v = __builtin_elementwise_fma(a, f0, c * f1);
        pk[r] = pkh(v.x, v.y);
      }
      const bool ev = (lane & 1) == 0;
      auto lo2 = [](u32 v) { return __builtin_amdgcn_perm(v, v, 0x01000100); };
      auto hi2 = [](u32 v) { return __builtin_amdgcn_perm(v, v, 0x03020302); };
      tp0 = pk[0];
      tp1 = ev ? lo2(pk[1]) : pk[1];
      tp2 = u32x2{lo2(pk[2]), ev ? pk[2] : hi2(pk[2])};
      tp3 = u32x4{lo2(pk[3]), lo2(pk[3]), ev ? pk[3] : hi2(pk[3]), hi2(pk[3])};
      asm volatile("" : "+v"(tp0), "+v"(tp1), "+v"(tp2), "+v"(tp3));
    }
    VMCNT(12); LGKM0();
    BAR();
    if (!helper) stage(5, sb + 2 * SLOT);
    {
      LDS char* ka = sm + L_KA + chl * 4160 + 64;
      *(LDS u32*)(ka + tt * 4) = tp0;
      *(LDS u32*)(ka + 512 + tt * 4) = tp1;
      *(LDS u32x2*)(ka + 1024 + tt * 8) = tp2;
      *(LDS u32x4*)(ka + 2048 + tt * 16) = tp3;
      if (tt < 16) *(LDS u32*)(ka - 64 + tt * 4) = 0u;
      if (tt < 4) *(LDS u32*)(sm + L_CAR + chl * 528 + tt * 4) = 0u;
    }
    VMCNT(12); LGKM0();
    BAR();
    if (!helper) { dma4_sc1(ws + WS_FS + (mt * 4 + q) * 128, sb + pollz, h * 4); stage(6, sb); }
    VMCNT(12);
    BAR();
    if (!helper) {
      if (!__all(*(LDS u32*)(sm + pollz + lane * 4) == tagw)) wait_flags(ws, WS_FS + (mt * 4 + q) * 128, true, tag);
      dma4_sc1(ws + WS_MEAN + (mt * 256 + q * 64) * 4, sb + L_MU + q * 256, lane * 4);
      stage(7, sb + SLOT);
    }
    VMCNT(12);
    STAMP(S_aB6);
    BAR();
    const u32 r2 = 2 * SLOT + q * 8192, r0 = q * 12288, r1 = SLOT + q * 8192;
    dma4_sc1(ws + WS_FS, sb + pollz, (lane & 31) * 128 + h * 4);
    wd2(0, r2); wd2(2, r2 + 4096);
    if (tt < 32) *(LDS u32x4*)(sm + L_U + chl * 4608 + tt * 16) = u32x4{0u, 0u, 0u, 0u};
    VMCNT(8);
    STAMP(S_aB7);
    BAR();
    if (!__all(*(LDS u32*)(sm + pollz + lane * 4) == tagw)) wait_flags(ws, WS_FS + (lane & 31) * 128, true, tag);
    STAMP(S_uchk);
    dma16x2_sc1(ws + WS_U + b * 8192 + q * 2048, sb + L_U + 512 + chl * 4608 + (q & 1) * 2048, lane * 16);
    const u32 qq = (lane & 15) >> 2;
    const u32 ta = (8 * h + qq) * 128 + (l4 & 1) * 32 + (lane & 3) * 8;
    LDS char* tb[2][2];
#pragma unroll
    for (int n = 0; n < 2; ++n) {
      tb[0][n] = sm + r0 + ta + ((((qq >> 1) & 1) ^ n) << 6);
      tb[1][n] = sm + r1 + ta + ((((qq >> 1) & 1) ^ n) << 6);
      asm volatile("" : "+v"(tb[0][n]), "+v"(tb[1][n]));
    }
    u32x4 wr[14][2];
    auto grab = [&](int j, u32 o) {
#pragma unroll
      for (int e = 0; e < 2; ++e)
#pragma unroll
        for (int n = 0; n < 2; ++n) wr[j + e][n] = trpair(tb[1][n] + SLOT + o + e * 2048, tb[1][n] + SLOT + o + e * 2048 + 512);
      LGKM0();
    };
    VMCNT(6); grab(0, 0);         wd2(4, r2);
    VMCNT(6); grab(2, 4096);  wd2(6, r2 + 4096);
    STAMP(S_aB8);
    BAR();
    VMCNT(4); grab(4, 0);         wd2(8, r2);
    VMCNT(4); grab(6, 4096);  wd2(10, r2 + 4096);
    STAMP(S_aB9);
    BAR();
    VMCNT(4); wd2(18, r0);         grab(8, 0);
    VMCNT(4); wd2(12, r2);         grab(10, 4096);
    STAMP(S_aB10);
    BAR();
    VMCNT(4); wd2(16, r2 + 4096);
    VMCNT(4); wd2(20, r0 + 4096);  grab(12, 0);
    VMCNT(4); wd2(14, r2);
    VMCNT(4); wd2(22, r0 + 8192);
    const u32 fh = WS_FH + (B128 * 2 + (q >> 1)) * 4096;
    const u32 fl = min(lane & 31, 27) * 128;
    VMCNT(4); dma4_sc1(ws + fh, sb + pollz, fl + h * 4); wd2(24, r1);
    VMCNT(5); wd2(26, r1 + 4096);
    STAMP(S_Wdone);
    VMCNT(8);
    if (!__all(*(LDS u32*)(sm + pollz + lane * 4) == tagw)) wait_flags(ws, fh + fl, true, tag);
    STAMP(S_hpollA);
    constexpr int PF = 9;
    u32x4 hf[56];
    const u32 hoff = WS_H + ((4 * B128 + sh) * 112 + q) * 1024;
    auto hload = [&](int i) {
      hf[i] = __builtin_amdgcn_raw_buffer_load_b128(wsr, lane * 16, hoff + (i / 28) * (2 * 112 * 1024) + (i % 28) * 4096, 16);
    };
#pragma unroll
    for (int i = 0; i < PF; ++i) { hload(i); SCHED(); }
    const u32 pw = (lane & 31) * 256;
#pragma unroll
    for (int m = 0; m < 2; ++m) {
      f32x16 acc0, acc1;
#pragma unroll
      for (int e = 0; e < 16; ++e) { acc0[e] = 0.f; acc1[e] = 0.f; }
      u32x4 wl[2][2];
#pragma unroll
      for (int j = 0; j < 28; ++j) {
        const int i = m * 28 + j;
        if (j + 1 >= 14 && j + 1 < 28) {
          const int u = j + 1 - 14;
          const u32 o = u < 4 ? SLOT + u * 2048 : u < 10 ? (u - 4) * 2048 : (u - 10) * 2048;
          const int rg = u >= 4 && u < 10 ? 0 : 1;
          wl[(j + 1) & 1][0] = trpair(tb[rg][0] + o, tb[rg][0] + o + 512);
          wl[(j + 1) & 1][1] = trpair(tb[rg][1] + o, tb[rg][1] + o + 512);
        }
        SCHED();
        acc0 = mma_bf(j < 14 ? wr[j < 14 ? j : 0][0] : wl[j & 1][0], hf[i], acc0);
        SCHED();
        if (i == 8) STAMP(S_h8);
        if (i + PF == 28 - PF - 1) dma4_sc1(ws + fh + 1024 * 128, sb + pollz, fl + h * 4);
        if (i + PF == 28) {
          if (!__all(*(LDS u32*)(sm + pollz + lane * 4) == tagw)) wait_flags(ws, fh + 1024 * 128 + fl, true, tag);
          STAMP(S_pollB);
        }
        if (i + PF < 56) hload(i + PF);
        SCHED();
        acc1 = mma_bf(j < 14 ? wr[j < 14 ? j : 0][1] : wl[j & 1][1], hf[i], acc1);
        SCHED();
      }
      STAMP(m ? S_Bmm : S_Amm);
      u32 pb;
      if (m == 0) {
        pb = q < 2 ? L_PA01 + q * 8192 : L_PA23 + (q - 2) * 8192;
        wait_lds_cnt(sm, L_MISC + 48, 2);
        wait_lds_cnt(sm, L_MISC + 52, 2);
      } else pb = r2;
#pragma unroll
      for (int g = 0; g < 4; ++g) {
        *(LDS f32x4*)(sm + pb + pw + (((2 * g + h) ^ (lane & 7)) << 4)) = f32x4{acc0[4 * g], acc0[4 * g + 1], acc0[4 * g + 2], acc0[4 * g + 3]};
        *(LDS f32x4*)(sm + pb + pw + 128 + (((2 * g + h) ^ (lane & 7)) << 4)) = f32x4{acc1[4 * g], acc1[4 * g + 1], acc1[4 * g + 2], acc1[4 * g + 3]};
      }
      LGKM0();
      STAMP(m ? S_B : S_A);
      if (m == 0) lds_inc(sm, L_MISC + 60, lane); else BAR();
    }
    STAMP_OUT();
    __builtin_amdgcn_endpgm();
  }
  STAMP_OUT();
}

__global__ void tiny_rec(char* ws) { if (threadIdx.x == 0) *(u64*)(ws + WS_STAMP) = __builtin_amdgcn_s_memrealtime(); }
__global__ void tiny_spin(char* ws) { const u64 r0 = *(volatile u64*)(ws + WS_STAMP); int n = 0; while (__builtin_amdgcn_s_memrealtime() - r0 < 10000 && ++n < 1000000) __builtin_amdgcn_s_sleep(1); }
__global__ void tiny_nop(char* ws) { }
extern "C" void kernel_launch(void* const* d_in, const int* in_sizes, int n_in, void* d_out, int out_size, void* d_ws, size_t ws_size,
                              hipStream_t stream) {
  fused_kernel<<<256, 512, 0, stream>>>((const u16*)d_in[0], (const u16*)d_in[1], (const u16*)d_in[2], (const u16*)d_in[3], (const u16*)d_in[4],
                                        (const u16*)d_in[5], (const u16*)d_in[6], (const u16*)d_in[8], (const u16*)d_in[9], (const u16*)d_in[10],
                                        (u16*)d_out, (char*)d_ws);
  tiny_nop<<<1, 64, 0, stream>>>((char*)d_ws);
}
```

```cpp
#include <hip/hip_runtime.h>
#include <hip/hip_bf16.h>

typedef unsigned u32;
typedef unsigned short u16;
typedef unsigned long long u64;
typedef u32 u32x2 __attribute__((ext_vector_type(2)));
typedef u32 u32x4 __attribute__((ext_vector_type(4)));
typedef float f32x2 __attribute__((ext_vector_type(2)));
typedef float f32x4 __attribute__((ext_vector_type(4)));
typedef float f32x16 __attribute__((ext_vector_type(16)));
typedef short s16x4 __attribute__((ext_vector_type(4)));
typedef __bf16 bf16x2 __attribute__((ext_vector_type(2)));
typedef __bf16 bf16x8 __attribute__((ext_vector_type(8)));
typedef _Float16 f16x2 __attribute__((ext_vector_type(2)));
typedef _Float16 f16x8 __attribute__((ext_vector_type(8)));
#define LDS __attribute__((address_space(3)))
#define GLB __attribute__((address_space(1)))
#define DEV __device__ __forceinline__

constexpr u32 WS_GEN = 0;
constexpr u32 WS_FG = 4096;
constexpr u32 WS_MEAN = WS_FG + 256 * 128;
constexpr u32 WS_RSTD = WS_MEAN + 8192;
constexpr u32 WS_CS = 512u << 10;
constexpr u32 WS_FLAG = 1u << 20, FCS = 131072 + 640, HCS = 262144 + 640;
constexpr u32 F_U = 0, F_Y = 576, F_M = 832;
constexpr u32 WS_FH = 14u << 20;
constexpr u32 US = 4096 + 128, YS = 65536 + 128;
constexpr u32 WS_U = 16u << 20;
constexpr u32 WS_Y = 3u << 20;
constexpr u32 WS_H = 6u << 20;
constexpr u32 WS_END = WS_H + 2048 * 1792 * 2;

constexpr u32 XS = 32768;
constexpr u32 L_W = 3 * XS;
constexpr u32 CE = 16384;
constexpr u32 L_ST = L_W;
constexpr u32 L_SP = L_W + 49152;
constexpr u32 L_POLL = L_SP;
constexpr u32 L_MU = L_SP + 2048;
constexpr u32 L_CAR = L_MU + 2048;
constexpr u32 L_MISC = L_SP + 5120;
constexpr u32 L_DUMMY = L_MISC + 256;
constexpr u32 L_KA = L_SP + 5632;
constexpr u32 L_XH = 81920;
constexpr u32 L_U = L_W + CE;
constexpr u32 L_PA = L_W + CE;
static_assert(L_DUMMY + 256 == L_KA && L_KA + 10752 == 163840 && L_CAR + 1024 <= L_MISC, "lds map");

constexpr u64 TAG_STEP = 0x9E3779B97F4A7C15ull;
constexpr int YGO = 22;
constexpr int PF2 = 4;
constexpr int PF2AT = 22;
constexpr int APOS = 3;
constexpr int WTH = 6;
constexpr int HPF = 8;
constexpr int CLATE = 36;
static_assert(WTH <= 8 && PF2AT >= 28 - HPF, "order of waits");
constexpr int CONV_LATE = CLATE;
constexpr int CB = 42752;
constexpr u32 CODE_BYTES = CB;
#define VMCNT(n) asm volatile("s_waitcnt vmcnt(%0)" ::"n"(n) : "memory")
#define LGKM0() asm volatile("s_waitcnt lgkmcnt(0)" ::: "memory")
#define BAR() __builtin_amdgcn_s_barrier()
#define SCHED() __builtin_amdgcn_sched_barrier(0)


DEV void dma16x4(const void* sb, u32 lds, u32 v0, u32 v1, u32 v2, u32 v3) {
  asm volatile("s_mov_b32 m0, %1\n\ts_nop 0\n\t"
               "global_load_lds_dwordx4 %2, %0\n\t"
               "global_load_lds_dwordx4 %3, %0 offset:1024\n\t"
               "global_load_lds_dwordx4 %4, %0 offset:2048\n\t"
               "global_load_lds_dwordx4 %5, %0 offset:3072" ::"s"(sb), "s"(__builtin_amdgcn_readfirstlane(lds)), "v"(v0), "v"(v1), "v"(v2), "v"(v3) : "memory");
}
DEV void dma16x2_sc1(const void* sb, u32 lds, u32 v0) {
  asm volatile("s_mov_b32 m0, %1\n\ts_nop 0\n\t"
               "global_load_lds_dwordx4 %2, %0 sc1\n\t"
               "global_load_lds_dwordx4 %2, %0 offset:1024 sc1" ::"s"(sb), "s"(__builtin_amdgcn_readfirstlane(lds)), "v"(v0) : "memory");
}
DEV void dma4(const void* sb, u32 lds, u32 v0) {
  asm volatile("s_mov_b32 m0, %1\n\ts_nop 0\n\tglobal_load_lds_dword %2, %0" ::"s"(sb), "s"(__builtin_amdgcn_readfirstlane(lds)), "v"(v0) : "memory");
}
template <int N> DEV void dma16_lanes(const char* ws, u32 off, u32 lds, u32 v0) {
  asm volatile("s_mov_b32 m0, %1\n\ts_mov_b32 exec_hi, 0\n\ts_mov_b32 exec_lo, %3\n\tglobal_load_lds_dwordx4 %2, %0 sc1\n\ts_mov_b64 exec, -1"
               ::"s"(ws + __builtin_amdgcn_readfirstlane(off)), "s"(__builtin_amdgcn_readfirstlane(lds)), "v"(v0), "n"((u32)((1ull << N) - 1)) : "memory");
}
DEV void st16_sc1(const void* sb, u32 voff, u32x4 v) {
  asm volatile("global_store_dwordx4 %0, %1, %2 sc1\n\ts_nop 1" ::"v"(voff), "v"(v), "s"(sb) : "memory");
}
DEV float bflo(u32 v) { return __builtin_bit_cast(float, v << 16); }
DEV float bfhi(u32 v) { return __builtin_bit_cast(float, v & 0xffff0000u); }
DEV u32 pkbf(float a, float b) { f32x2 v = {a, b}; return __builtin_bit_cast(u32, __builtin_convertvector(v, bf16x2)); }
DEV u32 pkh(float a, float b) { f32x2 v = {a, b}; return __builtin_bit_cast(u32, __builtin_convertvector(v, f16x2)); }
DEV float rbf(float a) { return bflo(pkbf(a, a)); }
template <int CTRL> DEV float dppf(float v) { return __builtin_bit_cast(float, __builtin_amdgcn_update_dpp(0, __builtin_bit_cast(int, v), CTRL, 0xf, 0xf, true)); }
template <int CTRL> DEV u32 dppu(u32 v) { return (u32)__builtin_amdgcn_update_dpp(0, (int)v, CTRL, 0xf, 0xf, true); }
DEV f32x2 cmul(f32x2 a, f32x2 b) {
  f32x2 t = f32x2{a.x, a.x} * b;
  return __builtin_elementwise_fma(f32x2{a.y, a.y}, f32x2{-b.y, b.x}, t);
}
DEV u32x4 trpair(LDS char* p0, LDS char* p1) {
  u32x2 a = __builtin_bit_cast(u32x2, __builtin_amdgcn_ds_read_tr16_b64_v4i16((LDS s16x4*)p0));
  u32x2 c = __builtin_bit_cast(u32x2, __builtin_amdgcn_ds_read_tr16_b64_v4i16((LDS s16x4*)p1));
  return u32x4{a.x, a.y, c.x, c.y};
}
DEV f32x16 mma_bf(u32x4 a, u32x4 b, f32x16 c) {
  return __builtin_amdgcn_mfma_f32_32x32x16_bf16(__builtin_bit_cast(bf16x8, a), __builtin_bit_cast(bf16x8, b), c, 0, 0, 0);
}
DEV f32x4 mma16(u32x4 a, u32x4 b, f32x4 c) {
  return __builtin_amdgcn_mfma_f32_16x16x32_bf16(__builtin_bit_cast(bf16x8, a), __builtin_bit_cast(bf16x8, b), c, 0, 0, 0);
}
DEV f32x4 mma16s(u32x2 a, u32x2 b, f32x4 c) {
  return __builtin_amdgcn_mfma_f32_16x16x16bf16_1k(__builtin_bit_cast(s16x4, a), __builtin_bit_cast(s16x4, b), c, 0, 0, 0);
}
DEV f32x16 mma_h(u32x4 a, u32x4 b, f32x16 c) {
  return __builtin_amdgcn_mfma_f32_32x32x16_f16(__builtin_bit_cast(f16x8, a), __builtin_bit_cast(f16x8, b), c, 0, 0, 0);
}
DEV void set_flag(char* ws, u32 off, u64 tag) {
  __hip_atomic_store((GLB u64*)(ws + off), tag, __ATOMIC_RELAXED, __HIP_MEMORY_SCOPE_AGENT);
}
DEV void post_flag(const char* ws, u32 f, u64 tag, int lane) {
  st16_sc1(ws + WS_FLAG + f * 128, (lane >> 3) * FCS + (lane & 7) * 16, u32x4{(u32)tag, (u32)(tag >> 32), (u32)tag, (u32)(tag >> 32)});
}
DEV u32 hflag_copy() { return WS_FH + (((u32)__builtin_amdgcn_s_memrealtime() >> 5) & 3) * HCS; }
DEV u32 flag_copy() { return WS_FLAG + (((u32)__builtin_amdgcn_s_memrealtime() >> 5) & 7) * FCS; }
template <int COPIES> DEV void wait_flags(char* ws, u32 off, bool act, u64 tag) {
#pragma unroll 1
  for (int spins = 0; spins < (1 << 18); ++spins) {
    const char* p = ws + (COPIES == 2 ? hflag_copy() : COPIES ? flag_copy() : 0u);
    if (act) act = __hip_atomic_load((GLB u64*)(p + off), __ATOMIC_RELAXED, __HIP_MEMORY_SCOPE_AGENT) != tag;
    if (!__any(act)) break;
  }
  asm volatile("" ::: "memory");
}
DEV u32 arrive(LDS char* sm, u32 off) {
  u32 r;
  asm volatile("s_mov_b64 exec, 1\n\tds_add_rtn_u32 %0, %1, %2\n\ts_mov_b64 exec, -1" : "=&v"(r) : "v"((u32)(size_t)sm + off), "v"(1u) : "memory");
  return r;
}
DEV u32 first_lane(u32 v) { u32 r; asm volatile("v_readfirstlane_b32 %0, %1" : "=s"(r) : "v"(v)); return r; }
DEV void wait_lds_cnt(LDS char* sm, u32 off, u32 want) {
#pragma unroll 1
  for (int spins = 0; spins < (1 << 24); ++spins) {
    u32 v = __hip_atomic_load((LDS u32*)(sm + off), __ATOMIC_RELAXED, __HIP_MEMORY_SCOPE_WORKGROUP);
    if (__builtin_amdgcn_readfirstlane(v) >= want) break;
  }
  asm volatile("" ::: "memory");
}
DEV void wait_lds_sleep(LDS char* sm, u32 off, u32 want) {
#pragma unroll 1
  for (int spins = 0; spins < (1 << 24); ++spins) {
    u32 v = __hip_atomic_load((LDS u32*)(sm + off), __ATOMIC_RELAXED, __HIP_MEMORY_SCOPE_WORKGROUP);
    if (__builtin_amdgcn_readfirstlane(v) >= want) break;
    __builtin_amdgcn_s_sleep(2);
  }
  asm volatile("" ::: "memory");
}
DEV void lds_inc(LDS char* sm, u32 off, int lane) {
  asm volatile("s_mov_b64 exec, 1\n\tds_add_u32 %0, %1\n\ts_mov_b64 exec, -1" ::"v"((u32)(size_t)sm + off), "v"(1u) : "memory");
}

template <int N> struct IC { static constexpr int v = N; };
template <int I, int N, class F> DEV void static_for(F&& f) {
  if constexpr (I < N) { f(IC<I>{}); static_for<I + 1, N>(f); }
}
template <int PS> struct ConvPlan {
  int t[72], k[72], acc[72], fresh[72], newk[72];
  constexpr ConvPlan() : t{}, k{}, acc{}, fresh{}, newk{} {
    const int T[4] = {PS ? 0 : 5, PS ? 1 : 2, PS ? 6 : 3, PS ? 7 : 4};
    const int ord[4] = {PS ? 2 : 0, PS ? 0 : 1, PS ? 1 : 2, 3};
    int cur[2] = {ord[0], ord[1]}, next = 2, done[4] = {0, 0, 0, 0}, used[5] = {0, 0, 0, 0, 0}, seen = 0;
    bool dual = false;
    for (int n = 0; n < 72; ++n) {
      const int s = n & 1;
      if (!dual && done[cur[s]] == 4 * T[cur[s]] + 4) {
        if (next < 4) cur[s] = ord[next++]; else { dual = true; cur[s] = 4; }
      }
      const int a = cur[s], i = a == 4 ? 3 : a;
      t[n] = i; k[n] = done[i]++; acc[n] = a; fresh[n] = !used[a]; used[a] = 1;
      newk[n] = k[n] >= seen; if (newk[n]) seen = k[n] + 1;
    }
  }
};
template <int PS, class Hook, class Store>
DEV void conv_half(LDS char* sm, LDS char* kp, LDS char* up, LDS char* car, u32 cnt, int lane, float ysc, Hook hook, Store store) {
  constexpr int T[4] = {PS ? 0 : 5, PS ? 1 : 2, PS ? 6 : 3, PS ? 7 : 4};
  constexpr ConvPlan<PS> P;
  constexpr bool CH[4] = {false, PS == 1, PS == 0, true};
  constexpr int H0[3] = {PS ? 10 : 49, PS ? 26 : 26, PS ? 57 : 61};
  constexpr int GV = PS ? 1 : 0, RC = PS ? 2 : 1;
  constexpr int RD = PS ? 9 : 16;
  constexpr int D = 4;
  const bool top = lane == 63;
  u32x4 kf[32], uf[72];
  f32x16 z[5], zero;
  float sel[2], cv, rcv;
  u32 yo[8];
#pragma unroll
  for (int e = 0; e < 16; ++e) zero[e] = 0.f;
  auto fetch = [&](int n) __attribute__((always_inline)) {
    const int k = P.k[n];
    if (P.newk[n]) kf[k] = trpair(kp + 128 * k + 32, kp + 128 * k);
    uf[n] = *(LDS u32x4*)(up + 384 + 512 * T[P.t[n]] - 128 * k);
  };
  auto pick = [&](int i, int st, int u) __attribute__((always_inline)) {
    const int c = 3 - (st >> 2), g = st & 3;
    if (g == 0 && !CH[i]) return;
    float pv = g ? z[i][4 * g - 4 + c] : z[i ? i - 1 : 0][12 + c];
    asm("" : "+v"(pv));
    sel[u] = top ? pv : z[i][4 * g + c];
  };
  auto add = [&](int i, int st, int u) __attribute__((always_inline)) {
    const int c = 3 - (st >> 2), g = st & 3;
    if (g == 0 && !CH[i]) z[i][c - 1] += dppf<0x138>(z[i][c]);
    else z[i][4 * g + c - 1] += dppf<0x13c>(sel[u]);
  };
  auto horner = [&](int i, int r) __attribute__((always_inline)) {
    if (r > 0) { add(i, 2 * r - 2, 0); add(i, 2 * r - 1, 1); }
    if (r < 6) { pick(i, 2 * r, 0); pick(i, 2 * r + 1, 1); }
  };
  auto carry = [&](int i, int r) __attribute__((always_inline)) {
    if (r > 0) cv = dppf<0x13c>(cv);
    if (r < 3) cv = top ? z[i][15 - r] : (r ? cv : 0.f);
  };
  auto pack = [&](int i, int p) __attribute__((always_inline)) { yo[2 * i + p] = pkbf(z[i][8 * p] * ysc, z[i][8 * p + 4] * ysc); asm volatile("" : "+v"(yo[2 * i + p])); };
#pragma unroll
  for (int n = 0; n < D; ++n) fetch(n);
  SCHED();
  static_for<0, 72>([&](auto nc) __attribute__((always_inline)) {
    constexpr int n = decltype(nc)::v;
    z[P.acc[n]] = mma_h(uf[n], kf[P.k[n]], P.fresh[n] ? zero : z[P.acc[n]]);
    SCHED();
    if (n + D < 72) fetch(n + D);
    static_for<0, 3>([&](auto jc) __attribute__((always_inline)) {
      constexpr int j = decltype(jc)::v;
      const int r = n - H0[j];
      if (r >= 0 && r < 7) horner(j, r);
      if (j == GV) {
        if (r >= 7 && r < 11) carry(j, r - 7);
        if (r == 10) *(LDS float*)(car + PS * 256 + lane * 4) = lane == 3 ? 1.f : cv;
        if (r == 11) pack(j, 1);
        if (r == 12 && PS) pack(j, 0);
      } else if (j == RC) {
        if (r == RD) {
          rcv = *(volatile LDS float*)(car + (PS ^ 1) * 256 + lane * 4);
        }
        if (r == 7) pack(j, 1);
        if (r == RD + 4) {
#pragma unroll 1
          for (int spins = 0; spins < (1 << 24) && __builtin_amdgcn_readlane(__builtin_bit_cast(int, rcv), 3) == 0; ++spins)
            rcv = *(volatile LDS float*)(car + (PS ^ 1) * 256 + lane * 4);
          z[j][0] += lane == 3 ? 0.f : rcv;
        }
        if (r == RD + 5) pack(j, 0);
      } else {
        if (r == 7) pack(j, 0);
        if (r == 8) pack(j, 1);
      }
    });
    hook(nc);
    SCHED();
  });
  lds_inc(sm, cnt + PS * 4, lane);
#pragma unroll
  for (int j = 0; j < 6; ++j) if (PS || j) store(2 * T[j >> 1] + (j & 1), yo[j]);
  z[3] += z[4];
#pragma unroll
  for (int r = 0; r < 7; ++r) horner(3, r);
  pack(3, 0); pack(3, 1);
  store(2 * T[3], yo[6]); store(2 * T[3] + 1, yo[7]);
  if (!PS) {
#pragma unroll
    for (int r = 0; r < 4; ++r) carry(3, r);
    z[0][0] += cv;
    pack(0, 0);
    store(2 * T[0], yo[0]);
  }
}

__global__ __launch_bounds__(512, 2) void fused_kernel(const u16* __restrict__ x, const u16* __restrict__ lar, const u16* __restrict__ aim,
                                                       const u16* __restrict__ cre, const u16* __restrict__ cim, const u16* __restrict__ ldt,
                                                       const u16* __restrict__ dsk, const u16* __restrict__ wg, const u16* __restrict__ wu,
                                                       const u16* __restrict__ wd, u16* __restrict__ out, char* ws) {
  __shared__ __attribute__((aligned(16))) char smem[163840];
  LDS char* sm = (LDS char*)smem;
  const u32 sb = (u32)(size_t)sm;
  {
    u64 pc0; asm volatile("s_getpc_b64 %0" : "=s"(pc0));
    dma4((const char*)(pc0 & ~127ull), sb + L_DUMMY, min(((blockIdx.x >> 3) << 10) + ((threadIdx.x >> 6) << 7) + ((threadIdx.x & 1) << 15), CODE_BYTES - 128u));
  }
  const int b = blockIdx.x, xcd = b & 7, idx = b >> 3;
  const int lane = threadIdx.x & 63;
  const int w = __builtin_amdgcn_readfirstlane(threadIdx.x >> 6) ^ 4, q = w & 3;
  const int a2 = xcd & 1, bq = xcd >> 1;
  const int mt = a2 * 4 + (idx & 3), ft = bq * 7 + (idx >> 2);
  const int s0 = (2 * bq + 4 * a2) & 7;
  const bool helper = idx >= 28;
  const int hb = a2 * 16 + bq * 4 + (idx & 3);
  const int rt = bq * 8 + (idx & 7), ct = a2 * 4 + (idx >> 3);
  const int B128 = rt >> 1, sh = rt & 1;
  const int h = lane >> 5;
  const int chl = q >> 1, ch = 2 * b + chl;
  const u32 cp = lane & 7, l3 = lane >> 3, l4 = lane >> 4;
  u32 vx[4], vw[4];
  auto make_v = [&](u32 ln) __attribute__((always_inline)) {
    vx[0] = (mt * 256 + 64 * q + (ln >> 3)) * 1024 + (((ln & 7) ^ (ln >> 4)) << 4);
    vw[0] = (32 * (q & 1) + (ln >> 3)) * 3584 + ft * 128 + (((ln & 7) ^ (((ln >> 4) & 1) << 1)) << 4);
    asm volatile("" : "+v"(vx[0]), "+v"(vw[0]));
    vx[1] = (vx[0] ^ 64) + 7168; vx[2] = vx[0] + 14336; vx[3] = vx[1] + 14336;
    vw[1] = (vw[0] ^ 64) + 27648; vw[2] = vw[0] + 2 * 27648; vw[3] = vw[1] + 2 * 27648;
  };
  const u32 xto = (mt * 256 + (idx >> 3) * 64) * 1024;
  u32 o0 = xto + (s0 + ((idx >> 2) & 1)) * 128, o1 = (((s0 + (idx & 3)) & 7) * 64) * 3584 + ft * 128, o3 = ct * 128;
  asm volatile("" : "+s"(o0), "+s"(o1), "+s"(o3));
  u32 vt = lane * (q == 0 ? 1024 : q == 3 ? 0 : 3584) + (q == 0 ? o0 : q == 3 ? o3 : o1);
  u32 vp = (b * 256 + (threadIdx.x & 255)) * 2, vl = ch * 2, vz = 0;
  asm volatile("" : "+v"(vt), "+v"(vp), "+v"(vl), "+v"(vz));
  asm volatile("" ::"s"(x), "s"(lar), "s"(aim), "s"(cre), "s"(cim), "s"(ldt), "s"(dsk), "s"(wg), "s"(wu), "s"(wd), "s"(out), "s"(ws));
  const char* wb = (const char*)((q & 2) ? wu : wg);
  auto stx = [&](int k) __attribute__((always_inline)) {
    const char* p = (const char*)x + ((k + s0) & 7) * 128;
    dma16x4(p, sb + (k % 3) * XS + q * 8192, vx[0], vx[1], vx[2], vx[3]);
    dma16x4(p + 32768, sb + (k % 3) * XS + q * 8192 + 4096, vx[0], vx[1], vx[2], vx[3]);
  };
  auto stw = [&](int k) __attribute__((always_inline)) { dma16x4(wb + ((k + s0) & 7) * (64 * 3584), sb + L_W + ((k + 1) % 3) * 16384 + q * 4096, vw[0], vw[1], vw[2], vw[3]); };
  u32 l_lar, l_aim, l_cr, l_ci, l_ldt;
  u64 l_gen;
  if (w >= 4)
    asm volatile("global_load_ushort %0, %6, %9\n\tglobal_load_ushort %1, %6, %10\n\tglobal_load_ushort %2, %6, %11\n\t"
                 "global_load_ushort %3, %6, %12\n\tglobal_load_ushort %4, %7, %13\n\tglobal_load_dwordx2 %5, %8, %14 sc1"
                 : "=&v"(l_lar), "=&v"(l_aim), "=&v"(l_cr), "=&v"(l_ci), "=&v"(l_ldt), "=&v"(l_gen)
                 : "v"(vp), "v"(vl), "v"(vz), "s"(lar), "s"(aim), "s"(cre), "s"(cim), "s"(ldt), "s"(ws) : "memory");
  if (helper) {
    const u32 vh = (hb * 64 + 32 * (w >> 2) + q) * 1024 + lane * 16;
    dma16x4(x, sb + L_XH + w * 8192, vh, vh + 3072, vh + 6144, vh + 9216);
    dma16x4((const char*)x + 16384, sb + L_XH + w * 8192 + 4096, vh, vh + 3072, vh + 6144, vh + 9216);
  }
  make_v(lane);
  if (w < 4 && !helper) stw(0);
  if (w < (helper ? 1 : 4)) dma4(q == 0 ? (const char*)x : q == 3 ? (const char*)dsk : wb, sb + L_DUMMY, vt);
  if (w < 4 && !helper) stx(0);
  SCHED();
  const auto wsr = __builtin_amdgcn_make_buffer_rsrc(ws, 0, WS_END, 0x00020000);

  if (helper) {
    u64 tag;
    asm volatile("s_waitcnt vmcnt(0)\n\tv_mov_b64 %0, %1" : "=&v"(tag) : "v"(l_gen) : "memory");
    u32x4 xr[8];
#pragma unroll
    for (int j = 0; j < 8; ++j) xr[j] = *(LDS u32x4*)(sm + L_XH + w * 8192 + j * 1024 + lane * 16);
    tag += TAG_STEP;
    float d[8][8], s[8];
#pragma unroll
    for (int j = 0; j < 8; ++j) {
#pragma unroll
      for (int e = 0; e < 4; ++e) { d[j][2 * e] = bflo(xr[j][e]); d[j][2 * e + 1] = bfhi(xr[j][e]); }
      const f32x2 t = (f32x2{d[j][0], d[j][1]} + f32x2{d[j][2], d[j][3]}) + (f32x2{d[j][4], d[j][5]} + f32x2{d[j][6], d[j][7]});
      s[j] = t.x + t.y;
    }
    asm volatile("" : "+v"(s[7]));
    auto red8 = [&](float (&v)[8], float (&u)[2]) __attribute__((always_inline)) {
      float t[4];
#pragma unroll
      for (int i = 0; i < 4; ++i) {
        auto r = __builtin_amdgcn_permlane32_swap(__builtin_bit_cast(u32, v[i]), __builtin_bit_cast(u32, v[i + 4]), false, false);
        t[i] = __builtin_bit_cast(float, (u32)r[0]) + __builtin_bit_cast(float, (u32)r[1]);
      }
#pragma unroll
      for (int i = 0; i < 2; ++i) {
        auto r = __builtin_amdgcn_permlane16_swap(__builtin_bit_cast(u32, t[i]), __builtin_bit_cast(u32, t[i + 2]), false, false);
        float z = __builtin_bit_cast(float, (u32)r[0]) + __builtin_bit_cast(float, (u32)r[1]);
        z += dppf<0x128>(z); z += dppf<0x124>(z); z += dppf<0x122>(z); z += dppf<0x121>(z);
        u[i] = z;
      }
    };
    auto bcast = [&](float (&u)[2], int j) __attribute__((always_inline)) { return __builtin_bit_cast(float, __builtin_amdgcn_readlane(__builtin_bit_cast(int, u[j & 1]), 16 * (j >> 1))); };
    float mean[2], var[2], rs[2], of[2];
    red8(s, mean);
    mean[0] *= (1.f / 512.f); mean[1] *= (1.f / 512.f);
    const int srow = 32 * (w >> 2) + 8 * (lane >> 4) + q;
    if ((lane & 15) == 0) { *(LDS float*)(sm + 65536 + srow * 4) = rbf(mean[0]); *(LDS float*)(sm + 65536 + srow * 4 + 16) = rbf(mean[1]); }
    if (threadIdx.x == 0) { *(LDS u64*)(sm + L_MISC) = tag; *(LDS u32*)(sm + L_MISC + 96) = 0u; }
    LGKM0();
    BAR();
    {
      tag = *(LDS u64*)(sm + L_MISC);
      const u64 gr = (tag & 0xffffffff0000ffffull) | (*(LDS u32*)(sm + 65536 + lane * 4) ^ 0x80000000u);
      asm volatile("global_store_dwordx2 %0, %1, %2 sc1" ::"v"(lane * 8), "v"(gr), "s"(ws + WS_FLAG + w * FCS + (F_M + hb * 4) * 128) : "memory");
      if (w < 7) dma4((const char*)wd + (w * 32 + hb) * 8192, sb + L_DUMMY, lane * 128);
    }
#pragma unroll
    for (int j = 0; j < 8; ++j) {
      const float m = bcast(mean, j);
      f32x2 a2v = {0.f, 0.f};
#pragma unroll
      for (int e = 0; e < 4; ++e) {
        const f32x2 t = f32x2{d[j][2 * e], d[j][2 * e + 1]} - f32x2{m, m};
        d[j][2 * e] = t.x; d[j][2 * e + 1] = t.y;
        a2v = __builtin_elementwise_fma(t, t, a2v);
      }
      s[j] = a2v.x + a2v.y;
    }
    red8(s, var);
#pragma unroll
    for (int i = 0; i < 2; ++i) {
      const float mb = rbf(mean[i]);
      rs[i] = __builtin_amdgcn_rcpf(rbf(__builtin_amdgcn_sqrtf(rbf(rbf(var[i] * (1.f / 512.f)) + 1e-5f))));
      of[i] = (mean[i] - mb) * rs[i];
      mean[i] = mb;
    }
    if ((lane & 15) == 0) { *(LDS float*)(sm + 65792 + srow * 4) = rs[0]; *(LDS float*)(sm + 65792 + srow * 4 + 16) = rs[1]; }
#pragma unroll
    for (int j = 0; j < 8; ++j) {
      const float r = bcast(rs, j), o = bcast(of, j);
#pragma unroll
      for (int e = 0; e < 4; ++e) {
        const f32x2 t = __builtin_elementwise_fma(f32x2{d[j][2 * e], d[j][2 * e + 1]}, f32x2{r, r}, f32x2{o, o});
        d[j][2 * e] = t.x; d[j][2 * e + 1] = t.y;
      }
    }
#pragma unroll
    for (int i = 0; i < 8; ++i) {
      const u32x4 o = {pkh(d[0][i], d[1][i]), pkh(d[2][i], d[3][i]), pkh(d[4][i], d[5][i]), pkh(d[6][i], d[7][i])};
      *(LDS u32x4*)(sm + (8 * lane + i) * 128 + ((w ^ (lane & 7)) << 4)) = o;
    }
    LGKM0();
    BAR();
  }
  SCHED();

  u32 vd[4];
  auto make_vd = [&]() __attribute__((always_inline)) {
#pragma unroll
    for (int j = 0; j < 4; ++j) vd[j] = l3 * 1024 + ((cp ^ (l4 << 1)) << 4) + 7168 * j;
  };
  const char* db = (const char*)wd + (q & 1) * 65536 + (q >> 1) * 32768 + ct * 128;
  auto wdg = [&](int g, int c) __attribute__((always_inline)) { dma16x4(db + g * 131072, sb + c * CE + q * 4096, vd[0], vd[1], vd[2], vd[3]); };
  const u32 rs = L_ST + q * 4096;

  if (helper && w < 4) {
      const u64 tag = *(LDS u64*)(sm + L_MISC);
      if (w == 0) {
        if (lane == 0) set_flag(ws, WS_FG + b * 8, tag);
        __builtin_amdgcn_raw_buffer_store_b32(*(LDS u32*)(sm + 65536 + lane * 4), wsr, lane * 4, WS_MEAN + hb * 256, 16);
        __builtin_amdgcn_raw_buffer_store_b32(*(LDS u32*)(sm + 65536 + 256 + lane * 4), wsr, lane * 4, WS_RSTD + hb * 256, 16);
      }
      u32x4 v[16];
#pragma unroll
      for (int it = 0; it < 16; ++it) v[it] = *(LDS u32x4*)(sm + (128 * w + 8 * it + (lane >> 3)) * 128 + (((lane & 7) ^ (it & 7)) << 4));
#pragma unroll
      for (int it = 0; it < 16; ++it) st16_sc1(ws + WS_U + (128 * w + 8 * it) * US + hb * 128, (lane >> 3) * US + (lane & 7) * 16, v[it]);
      make_vd();
      VMCNT(0);
      {
        const u32 arr = arrive(sm, L_MISC + 96);
        LGKM0();
        if (first_lane(arr) == 3) post_flag(ws, F_U + hb, tag, lane);
      }
      wdg(0, 4); wdg(1, 5); wdg(6, 6);
      BAR(); BAR();
      wdg(2, 0);
      BAR();
      wdg(3, 1);
      BAR();
      wdg(4, 2);
      BAR(); BAR(); BAR(); BAR();
      wdg(5, 3);
      if (b == 255) wait_flags<0>(ws, WS_FG + (w * 64 + lane) * 8, true, tag);
      VMCNT(0);
      BAR();
      if (b == 255 && w == 0 && lane == 0) set_flag(ws, WS_GEN, tag);
  }
  SCHED();

  if (w < 4) {
    __builtin_amdgcn_s_setprio(1);
    LDS char* kp = sm + L_KA + chl * 4160 + 32 + (32 * h + 16 * ((lane >> 4) & 1) + 4 * (lane & 3) - 4 * ((lane & 15) >> 2)) * 2;
    LDS char* up = sm + L_U + chl * 4608 + 128 + (lane & 31) * 16 - 64 * h;
    u32 yv = h * YS + (lane & 31) * 4;
    asm volatile("" : "+v"(kp), "+v"(up), "+v"(yv));
    const char* hb0 = ws + WS_H + (((mt * 8 + (w >> 1) * 4) * 28 + ft) * 4 + 2 * (w & 1)) * 1024;
    u32 hv = lane * 16;
    u32 fv = (lane >> 4) * HCS + (lane & 15) * 8;
    asm volatile("" : "+v"(hv), "+v"(fv));
    const char* fhp = ws + WS_FH + ((mt * 4 + w) * 32 + ft) * 128;
    asm volatile("" : "+s"(fhp));
    auto hpost = [&](u32 o, u64 t) __attribute__((always_inline)) { asm volatile("global_store_dwordx2 %0, %1, %2 sc1" ::"v"(fv), "v"(t), "s"(fhp + o) : "memory"); };
    u32x4 ho[4];
    u64 tag;
    float ysc;
    f32x2 wsc;
    LDS char* mp = sm + L_MISC + chl * 8;
    asm volatile("" : "+v"(mp));
    auto misc = [&](int i) __attribute__((always_inline)) {
      if (i == 0) tag = *(LDS u64*)(sm + L_MISC);
      if (i == 1) ysc = *(LDS float*)(mp + 24);
      if (i == 2) wsc = *(LDS f32x2*)(mp + 8);
      if (i == 3) { ysc = 2.f * ysc * fmaxf(wsc.x, wsc.y); asm volatile("" : "+v"(tag), "+v"(ysc)); }
    };
    if (__builtin_expect(!helper, 1)) {
      const int mh = w >> 1, fp = w & 1;
      const u32 r15 = lane & 15, qq = r15 >> 2;
      LDS char* xa[2][2];
      LDS char* wa[2];
#pragma unroll
      for (int i = 0; i < 2; ++i) {
        xa[0][i] = sm + (mh * 128 + r15) * 128 + (((4 * i + l4) ^ (r15 >> 1)) << 4);
        xa[1][i] = xa[0][i] + 2 * XS;
        wa[i] = sm + L_W + (8 * l4 + qq) * 128 + (((2 * fp + i) ^ (((qq >> 1) & 1) | ((l4 & 1) << 1))) << 5) + (lane & 3) * 8;
        asm volatile("" : "+v"(xa[0][i]), "+v"(xa[1][i]), "+v"(wa[i]));
      }
      LDS char* csr = sm + L_ST + l4 * 4096 + fp * 512 + r15 * 8;
      LDS char* mur = sm + L_MU + (mh * 128 + r15) * 8;
      asm volatile("" : "+v"(csr), "+v"(mur));
      { u32 ln = lane; asm volatile("" : "+v"(ln)); make_v(ln); }
      stw(1); stx(1);
      f32x4 acc[8][4];
      const f32x4 zero = {0.f, 0.f, 0.f, 0.f};
      u32x4 xf[128];
      u32x2 wf[16][4][2], ca[4], mb[8];
      u32 mu[8];
      auto rdw = [&](int st, int r) __attribute__((always_inline)) {
        const int j = r >> 1, t = r & 1;
        wf[st][j][t] = __builtin_bit_cast(u32x2, __builtin_amdgcn_ds_read_tr16_b64_v4i16((LDS s16x4*)(wa[j & 1] + (((st >> 1) + 1) % 3) * 16384 + (j >> 1) * 8192 + (st & 1) * 4096 + t * 512)));
      };
      auto rdx = [&](int st, int i) __attribute__((always_inline)) { const int k = st >> 1; xf[8 * st + i] = *(LDS u32x4*)(xa[k % 3 == 2][st & 1] + (k % 3 == 1 ? XS : 0) + i * 2048); };
      auto mm = [&](int st, int i, int j) __attribute__((always_inline)) {
        acc[i][j] = mma16(u32x4{wf[st][j][0].x, wf[st][j][0].y, wf[st][j][1].x, wf[st][j][1].y}, xf[8 * st + i], st == 0 ? zero : acc[i][j]);
      };
      float gp[4];
      auto gate = [&](int i, int v) __attribute__((always_inline)) {
        const int j = v / 3, u = v % 3;
        if (v == 6) { if (i < 4) st16_sc1(hb0 + (i >> 1) * (28 * 4096) + (i & 1) * 1024, hv, ho[i & 3]); }
        else if (u < 2) {
          gp[2 * u] = acc[i][j][2 * u] * acc[i][2 + j][2 * u];
          asm volatile("" : "+v"(gp[2 * u]));
          gp[2 * u + 1] = acc[i][j][2 * u + 1] * acc[i][2 + j][2 * u + 1];
          asm volatile("" : "+v"(gp[2 * u + 1]));
        } else { ho[i & 3][2 * j] = pkbf(gp[0], gp[1]); ho[i & 3][2 * j + 1] = pkbf(gp[2], gp[3]); }
      };
      auto mneg = [&](int i) __attribute__((always_inline)) { const u32 m = __builtin_amdgcn_perm(mu[i], mu[i], 0x03020302); mb[i] = u32x2{m, m}; };
      VMCNT(12);
      BAR();
      rdw(0, 0); rdw(0, 1); rdx(0, 0);
#pragma unroll
      for (int r = 2; r < 8; ++r) rdw(0, r);
      rdx(0, 1); rdx(0, 2); rdx(0, 3);
      SCHED();
      static_for<0, 14>([&](auto sc) __attribute__((always_inline)) {
        constexpr int st = decltype(sc)::v, k = st >> 1;
        constexpr bool last = st & 1;
#pragma unroll
        for (int g = 0; g < 32; ++g) {
          if (last && g == 14) {
            if (k == 0) VMCNT(0);
            __builtin_amdgcn_s_waitcnt(0xC07F); BAR();
            SCHED();
          }
          mm(st, g >> 2, (g & 4) ? 3 - (g & 3) : g & 3);
          SCHED();
          if (!last) {
            if ((g & 3) == 0) { if (g < 16) rdx(st, (g >> 2) + 4); else rdx(st + 1, (g >> 2) - 4); }
            if ((g & 3) == 2) rdw(st + 1, g >> 2);
          } else {
            if (g < 8 && (g & 1) == 0) rdx(st, (g >> 1) + 4);
            if (st < 13) {
              if (g >= 14 && g < 22) rdw(st + 1, g - 14);
              if (g >= 22 && g < 30 && (g & 1) == 0) rdx(st + 1, (g - 22) >> 1);
            } else if (g >= 14) {
              if (g < 18) { ca[g - 14].x = *(LDS u32*)(csr + ((g - 14) & 1) * 256 + ((g - 14) >> 1) * 128); ca[g - 14].y = *(LDS u32*)(csr + ((g - 14) & 1) * 256 + ((g - 14) >> 1) * 128 + 1024); }
              else if (g < 22) { mu[2 * g - 36] = *(LDS u32*)(mur + (2 * g - 36) * 128); mu[2 * g - 35] = *(LDS u32*)(mur + (2 * g - 35) * 128); }
              else if (g < 30) rdw(14, g - 22);
              else rdx(14 + (g & 1), 0);
              if (g == 28) mneg(0);
            }
          }
          if (st == 6 && g < 3) misc(g);
          if (st == 7 && g == 20) misc(3);
          SCHED();
        }
      });
      static_for<0, 8>([&](auto ic) __attribute__((always_inline)) {
        constexpr int i = decltype(ic)::v;
#pragma unroll
        for (int g = 0; g < 12; ++g) {
          if (i == 5 && g == 9) { __builtin_amdgcn_s_waitcnt(0xC07F); BAR(); SCHED(); }
          if (g < 4) acc[i][g] = mma16s(ca[g], mb[i], acc[i][g]); else mm(14 + (g >> 3), i, g & 3);
          SCHED();
          if (i == 0) {
            if (g < 4) { rdw(15, 2 * g); rdw(15, 2 * g + 1); }
            else if (g < 8) rdx(14 + (g & 1), 1 + ((g - 4) >> 1));
          }
          if (i > 0 && i < 6 && g < 2) rdx(14 + g, i + 2);
          if (i < 7 && g == (i ? 2 : 10)) mneg(i + 1);
          if (i > 0 && g >= 5) gate(i - 1, g - 5);
          SCHED();
        }
      });
#pragma unroll
      for (int e = 0; e < 4; ++e) ho[3][e] = pkbf(acc[7][e >> 1][2 * (e & 1)] * acc[7][2 + (e >> 1)][2 * (e & 1)], acc[7][e >> 1][2 * (e & 1) + 1] * acc[7][2 + (e >> 1)][2 * (e & 1) + 1]);
      VMCNT(0);
      hpost(0, tag);
#pragma unroll
      for (int d = 0; d < 4; ++d) st16_sc1(hb0 + (2 + (d >> 1)) * (28 * 4096) + (d & 1) * 1024, hv, ho[d]);
    } else {
      misc(0); misc(1); misc(2); misc(3);
    }

    BAR();
    const int t = threadIdx.x & 255, sr = t >> 3, dg = t & 7;
    const int rowA = 128 * B128 + 32 * sh + sr;
    {
      auto hook = [&](auto ic) __attribute__((always_inline)) {
        constexpr int n = decltype(ic)::v;
        if (n == CONV_LATE) {
          if (!helper) { VMCNT(0); hpost(131072, tag); }
        }
      };
      const char* yb = ws + WS_Y + ch * 128;
      auto store = [&](int blk, u32 v) __attribute__((always_inline)) { asm volatile("global_store_dword %0, %1, %2 sc1" ::"v"(yv), "v"(v), "s"(yb + blk * (2 * YS)) : "memory"); };
      if (w & 1) conv_half<1>(sm, kp, up, sm + L_CAR + chl * 512, L_MISC + 48 + chl * 8, lane, ysc, hook, store);
      else conv_half<0>(sm, kp, up, sm + L_CAR + chl * 512, L_MISC + 48 + chl * 8, lane, ysc, hook, store);
    }
    VMCNT(0);
    {
      const u32 arr = arrive(sm, L_MISC + 76);
      LGKM0();
      if (first_lane(arr) == 3) post_flag(ws, F_Y + b, tag, lane);
    }
    SCHED();
    const u32x4 xA = *(const u32x4*)((const char*)x + (u32)(rowA * 1024 + ct * 128 + dg * 16));
    const u32x4 xB = *(const u32x4*)((const char*)x + (u32)((rowA + 64) * 1024 + ct * 128 + dg * 16));
    const u32x4 dv = *(const u32x4*)((const char*)dsk + (u32)(ct * 128 + dg * 16));
    float mm[2], rr[2];
#pragma unroll
    for (int m = 0; m < 2; ++m) {
      mm[m] = __builtin_bit_cast(float, __builtin_amdgcn_raw_buffer_load_b32(wsr, (rowA + 64 * m) * 4, WS_MEAN, 16));
      rr[m] = __builtin_bit_cast(float, __builtin_amdgcn_raw_buffer_load_b32(wsr, (rowA + 64 * m) * 4, WS_RSTD, 16));
    }
    f32x2 base[2][4], r2[2];
#pragma unroll
    for (int m = 0; m < 2; ++m) {
      r2[m] = f32x2{rr[m] * rr[m], rr[m] * rr[m]};
#pragma unroll
      for (int e = 0; e < 4; ++e) {
        const u32 xv = m ? xB[e] : xA[e];
        base[m][e] = f32x2{fmaf((bflo(xv) - mm[m]) * rr[m], bflo(dv[e]), bflo(xv)), fmaf((bfhi(xv) - mm[m]) * rr[m], bfhi(dv[e]), bfhi(xv))};
      }
    }
    const u32 cl = (w & 1) * 16 + (lane >> 3), sq = lane & 7;
    LDS char* yw0 = sm + L_KA + chl * 4160 + sq * 512 + (((cl >> 2) ^ sq) << 4) + (cl & 3) * 4;
    LDS char* yw1 = sm + L_KA + chl * 4160 + sq * 512 + ((((cl + 8) >> 2) ^ sq) << 4) + (cl & 3) * 4;
    LDS char* yr0 = sm + L_KA + (dg >> 2) * 4160 + sr * 128 + (((2 * (dg & 3)) ^ (sr >> 2)) << 4);
    LDS char* yr1 = sm + L_KA + (dg >> 2) * 4160 + sr * 128 + (((2 * (dg & 3) + 1) ^ (sr >> 2)) << 4);
    LDS char* pp0 = sm + 5 * CE + (sr >> 4) * CE + (sr & 15) * 256 + (((2 * dg) ^ (sr & 7)) << 4);
    LDS char* pp1 = sm + 5 * CE + (sr >> 4) * CE + (sr & 15) * 256 + (((2 * dg + 1) ^ (sr & 7)) << 4);
    u32 ov = rowA * 1024 + ct * 128 + dg * 16;
    asm volatile("" : "+v"(yw0), "+v"(yw1), "+v"(yr0), "+v"(yr1), "+v"(pp0), "+v"(pp1), "+v"(ov), "+v"(r2[0]), "+v"(r2[1]));
#pragma unroll
    for (int e = 0; e < 4; ++e) asm volatile("" : "+v"(base[0][e]), "+v"(base[1][e]));
    auto addp = [&](int m) __attribute__((always_inline)) {
      f32x4 p[4][2];
#pragma unroll
      for (int k = 0; k < 4; ++k) {
        p[k][0] = *(LDS f32x4*)(pp0 + (m ? 0 : L_PA - 5 * CE) + k * 4096);
        p[k][1] = *(LDS f32x4*)(pp1 + (m ? 0 : L_PA - 5 * CE) + k * 4096);
      }
#pragma unroll
      for (int e = 0; e < 2; ++e) {
        const f32x4 t = (p[0][e] + p[1][e]) + (p[2][e] + p[3][e]);
        base[m][2 * e] = __builtin_elementwise_fma(f32x2{t[0], t[1]}, r2[m], base[m][2 * e]);
        base[m][2 * e + 1] = __builtin_elementwise_fma(f32x2{t[2], t[3]}, r2[m], base[m][2 * e + 1]);
      }
    };
    auto emit = [&](int m) __attribute__((always_inline)) {
      const u32x4 o = {pkbf(base[m][0].x, base[m][0].y), pkbf(base[m][1].x, base[m][1].y), pkbf(base[m][2].x, base[m][2].y), pkbf(base[m][3].x, base[m][3].y)};
      st16_sc1((char*)out + m * 65536, ov, o);
    };
    wait_lds_cnt(sm, L_MISC + 48 + (w ^ 1) * 4, 1);
    wait_lds_sleep(sm, L_MISC + 80, 1);
    wait_flags<1>(ws, (F_Y + ct * 32 + 8 * w + lane) * 128, lane < 8, tag);
    {
      const u32 yo = WS_Y + rt * YS + (ct * 64 + 16 * w) * 128;
      const u32x4 y0 = __builtin_amdgcn_raw_buffer_load_b128(wsr, lane * 16, yo, 16);
      const u32x4 y1 = __builtin_amdgcn_raw_buffer_load_b128(wsr, lane * 16, yo + 1024, 16);
#pragma unroll
      for (int e = 0; e < 4; ++e) { *(LDS u32*)(yw0 + e * 128) = y0[e]; *(LDS u32*)(yw1 + e * 128) = y1[e]; }
    }
    lds_inc(sm, L_MISC + 72, lane);
    wait_lds_sleep(sm, L_MISC + 64, 4);
    addp(0);
#pragma unroll
    for (int e = 0; e < 4; ++e) asm volatile("" : "+v"(base[0][e]));
    wait_lds_sleep(sm, L_MISC + 72, 4);
    {
      const u32x4 ya = *(LDS u32x4*)yr0, yb = *(LDS u32x4*)yr1;
#pragma unroll
      for (int e = 0; e < 4; ++e) {
        const u32 v0 = e < 2 ? ya[2 * e] : yb[2 * e - 4], v1 = e < 2 ? ya[2 * e + 1] : yb[2 * e - 3];
        base[0][e] += f32x2{bflo(v0), bflo(v1)};
        base[1][e] += f32x2{bfhi(v0), bfhi(v1)};
      }
    }
    emit(0);
#pragma unroll
    for (int e = 0; e < 4; ++e) asm volatile("" : "+v"(base[1][e]));
    BAR();
    addp(1);
    emit(1);
    __builtin_amdgcn_endpgm();
  }
  {
    u32x4 sel[2];
#pragma unroll
    for (int i = 0; i < 2; ++i) {
#pragma unroll
      for (int e = 0; e < 4; ++e) sel[i][e] = ((lane & 31) >> 1) == 8 * i + 4 * h + e ? ((lane & 1) ? 0x3c000000u : 0x00003c00u) : 0u;
      asm volatile("" : "+v"(sel[i]));
    }
    if (lane < 12) *(LDS u32*)(sm + L_MISC + 48 + lane * 4) = 0u;
    *(LDS u32*)(sm + L_CAR + q * 256 + lane * 4) = 0u;
    u32 p_lar, p_aim, p_cr, p_ci, p_ldt;
    u64 gen;
    if (!helper) VMCNT(0);
    asm volatile("v_lshlrev_b32 %0, 16, %6\n\tv_lshlrev_b32 %1, 16, %7\n\tv_lshlrev_b32 %2, 16, %8\n\t"
                 "v_lshlrev_b32 %3, 16, %9\n\tv_lshlrev_b32 %4, 16, %10\n\tv_mov_b64 %5, %11"
                 : "=&v"(p_lar), "=&v"(p_aim), "=&v"(p_cr), "=&v"(p_ci), "=&v"(p_ldt), "=&v"(gen)
                 : "v"(l_lar), "v"(l_aim), "v"(l_cr), "v"(l_ci), "v"(l_ldt), "v"(l_gen) : "memory");
    const u64 tag = gen + TAG_STEP;
      float dt = rbf(__expf(__builtin_bit_cast(float, p_ldt)));
      const float zr = -rbf(__expf(__builtin_bit_cast(float, p_lar))) * dt * 1.44269504f, zi = __builtin_bit_cast(float, p_aim) * dt * 0.15915494f;
      auto cexp = [&](float p, float sg) __attribute__((always_inline)) {
        const float mg = __builtin_amdgcn_exp2f(zr * p), t = __builtin_amdgcn_fractf(zi * p);
        return f32x2{mg * __builtin_amdgcn_cosf(t), sg * mg * __builtin_amdgcn_sinf(t)};
      };
      u32 mx = max(p_cr & 0x7fffffffu, p_ci & 0x7fffffffu);
      mx = max(mx, dppu<0x121>(mx)); mx = max(mx, dppu<0x122>(mx)); mx = max(mx, dppu<0x124>(mx)); mx = max(mx, dppu<0x128>(mx));
      u32 ex = max(max(__builtin_amdgcn_readlane(mx, 0), __builtin_amdgcn_readlane(mx, 16)), max(__builtin_amdgcn_readlane(mx, 32), __builtin_amdgcn_readlane(mx, 48)));
      ex = min(max(ex >> 23, 1u), 253u);
      const float isc = __builtin_bit_cast(float, (254u - ex) << 23);
      if (lane == 0) {
        *(LDS u32*)(sm + L_MISC + 8 + q * 4) = ex << 23;
        *(LDS float*)(sm + L_MISC + 24 + chl * 8) = dt;
        *(LDS u64*)(sm + L_MISC) = tag;
      }
      f32x2 V[56];
      const f32x2 w8 = cexp(8.f, 1.f), w64 = cexp(64.f, 1.f), w1 = cexp(1.f, -1.f), w256 = cexp(256.f, -1.f);
      V[0] = f32x2{1.f, 0.f}; V[1] = w8; V[2] = cmul(w8, w8); V[3] = cmul(V[2], w8); V[4] = cmul(V[2], V[2]);
      V[5] = cmul(V[1], V[4]); V[6] = cmul(V[2], V[4]); V[7] = cmul(V[3], V[4]);
      V[8] = w64; V[16] = cmul(w64, w64); V[24] = cmul(V[16], w64);
#pragma unroll
      for (int i = 1; i < 4; ++i)
#pragma unroll
        for (int j = 1; j < 8; ++j) V[8 * i + j] = cmul(V[j], V[8 * i]);
      f32x2* A = V + 32;
      A[0] = f32x2{__builtin_bit_cast(float, p_cr) * isc, -__builtin_bit_cast(float, p_ci) * isc};
      const f32x2 w2 = cmul(w1, w1), w4 = cmul(w2, w2), w512 = cmul(w256, w256);
      A[1] = cmul(A[0], w1); A[2] = cmul(A[0], w2); A[3] = cmul(A[1], w2);
#pragma unroll
      for (int j = 0; j < 4; ++j) A[4 + j] = cmul(A[j], w4);
#pragma unroll
      for (int j = 0; j < 4; ++j) A[8 + j] = cmul(A[2 * j], w256);
      A[12] = cmul(A[0], w512); A[13] = cmul(A[4], w512); A[14] = cmul(A[8], w512); A[15] = cmul(A[10], w512);
      A[16] = cmul(A[12], w512); A[17] = cmul(A[14], w512); A[18] = cmul(A[16], w512); A[19] = cmul(A[17], w512); A[20] = cmul(A[18], w512);
      A[21] = A[20]; A[22] = A[20]; A[23] = A[20];
      u32x4 fp[2][8];
#pragma unroll
      for (int g = 0; g < 7; ++g)
#pragma unroll
        for (int e = 0; e < 4; ++e) {
          fp[0][g][e] = pkh(V[8 * g + 2 * e].x, V[8 * g + 2 * e + 1].x);
          fp[1][g][e] = pkh(V[8 * g + 2 * e].y, V[8 * g + 2 * e + 1].y);
        }
      fp[0][7] = fp[0][6]; fp[1][7] = fp[1][6];
#pragma unroll
      for (int g = 0; g < 8; g += 2) asm volatile("" : "+v"(fp[0][g]), "+v"(fp[0][g + 1]), "+v"(fp[1][g]), "+v"(fp[1][g + 1]));
      f32x16 kk, zero, tq[8];
#pragma unroll
      for (int e = 0; e < 16; ++e) zero[e] = 0.f;
      u32x4 fq[16];
      auto swp = [&](int c, int g) __attribute__((always_inline)) {
#pragma unroll
        for (int e = 0; e < 4; ++e) {
          auto r = __builtin_amdgcn_permlane32_swap(fp[c][g][e], fp[c][g + 1][e], false, false);
          fp[c][g][e] = r[0]; fp[c][g + 1][e] = r[1];
        }
      };
      auto tmm = [&](int t, int p) __attribute__((always_inline)) { tq[t] = mma_h(fp[t >> 2][4 * (t & 1) + 2 * p + ((t >> 1) & 1)], sel[p], p ? tq[t] : zero); };
      auto cvt = [&](int t, int u) __attribute__((always_inline)) {
#pragma unroll
        for (int e = 0; e < 4; ++e) fq[2 * t + u][e] = pkh(tq[t][8 * u + 2 * e], tq[t][8 * u + 2 * e + 1]);
      };
      auto kmm = [&](int v) __attribute__((always_inline)) { kk = mma_h(fq[4 * (v >> 1) + 2 + (v & 1)], fq[4 * (v >> 1) + (v & 1)], v ? kk : zero); };
      swp(0, 0); SCHED();
      tmm(0, 0); swp(0, 4); SCHED();
      tmm(2, 0); swp(0, 2); SCHED();
      tmm(1, 0); swp(0, 6); SCHED();
      tmm(0, 1); swp(1, 0); SCHED();
      tmm(1, 1); swp(1, 4); SCHED();
      tmm(3, 0); swp(1, 2); SCHED();
      tmm(2, 1); swp(1, 6); SCHED();
      tmm(3, 1); cvt(0, 0); SCHED();
      tmm(4, 0); cvt(1, 0); SCHED();
      tmm(5, 0); cvt(0, 1); SCHED();
      kmm(0);    cvt(1, 1); SCHED();
      tmm(4, 1); cvt(2, 0); SCHED();
      tmm(5, 1); cvt(3, 0); SCHED();
      kmm(1);    cvt(2, 1); SCHED();
      tmm(6, 0); cvt(3, 1); SCHED();
      tmm(7, 0); cvt(4, 0); SCHED();
      kmm(2);    cvt(5, 0); SCHED();
      tmm(6, 1); cvt(4, 1); SCHED();
      tmm(7, 1); cvt(5, 1); SCHED();
      kmm(3); kmm(4); kmm(5); SCHED();
      cvt(6, 0); cvt(6, 1); cvt(7, 0); cvt(7, 1); SCHED();
      kmm(6); kmm(7); SCHED();
      BAR();
      if (!helper) stw(2);
    const u32 qq = (lane & 15) >> 2;
    LDS char* cwa = sm + L_W + (l4 & 1) * 8192 + (8 * h + qq) * 128 + ((q ^ (((qq >> 1) & 1) | (h << 1))) << 5) + (lane & 3) * 8 + (idx & 3) * 2048;
    asm volatile("" : "+v"(cwa));
    u32x4 cf[4];
    auto cread = [&](int k) __attribute__((always_inline)) { cf[k] = trpair(cwa + ((k + 1) % 3) * 16384, cwa + ((k + 1) % 3) * 16384 + 512); };
      cread(0);
      if (!helper) stx(2);
    const u32 tt = (q & 1) * 64 + lane;
    LDS char* pw0 = sm + L_KA + q * 2688 + lane * 16;
    LDS char* pr[6];
    {
      LDS char* pb = sm + L_KA + chl * 5376;
      const u32 t1 = tt + 1;
      pr[0] = pb + ((tt >> 1) & 1) * 512 + (tt >> 2) * 16 + (tt & 1) * 8;
      pr[1] = pb + 1024 + tt * 4;
      pr[2] = pb + 1536 + ((tt & 63) >> 1) * 16 + (tt >> 6) * 8 + (tt & 1) * 4;
      pr[3] = pb + (t1 == 128 ? 2048 : 1536 + ((t1 & 63) >> 1) * 16 + (t1 >> 6) * 8 + (t1 & 1) * 4);
      pr[4] = pb + 2048 + (tt & 31) * 16 + (tt >> 5) * 4;
      pr[5] = pb + (t1 == 128 ? 2560 : 2048 + (t1 & 31) * 16 + (t1 >> 5) * 4);
      asm volatile("" : "+v"(pw0), "+v"(pr[0]), "+v"(pr[1]), "+v"(pr[2]), "+v"(pr[3]), "+v"(pr[4]), "+v"(pr[5]));
    }
      LGKM0();
      BAR();
      cread(1);
      VMCNT(0);
      *(LDS f32x4*)pw0 = f32x4{kk[0], kk[1], kk[2], kk[3]};
      *(LDS f32x4*)(pw0 + 1024) = f32x4{kk[4], kk[5], kk[6], kk[7]};
      if (lane < 33) *(LDS f32x4*)(pw0 + 2048) = f32x4{kk[8], kk[9], kk[10], kk[11]};
      if (!helper) {
        if (w == 4 && lane == 0) set_flag(ws, WS_FG + b * 8, tag);
        stw(3); stx(3);
      }
    VMCNT(12); LGKM0();
    BAR();
    cread(2);
    if (!helper) { stw(4); stx(4); }
    LGKM0();
    float pa[4][4], es[2];
    {
      es[0] = *(LDS float*)(sm + L_MISC + 8 + chl * 8); es[1] = *(LDS float*)(sm + L_MISC + 12 + chl * 8);
#pragma unroll
      for (int r = 0; r < 4; ++r) {
        LDS char* p0 = pr[r < 2 ? r : 2 * r - 2];
        LDS char* p1 = r < 2 ? p0 + 4 : pr[2 * r - 1];
        pa[r][0] = *(LDS float*)p0; pa[r][1] = *(LDS float*)p1;
        pa[r][2] = *(LDS float*)(p0 + 2688); pa[r][3] = *(LDS float*)(p1 + 2688);
      }
    }
    VMCNT(12);
    BAR();
    cread(3);
    if (!helper) {
      stw(5);
      f32x16 cz;
#pragma unroll
      for (int e = 0; e < 16; ++e) cz[e] = 0.f;
      __builtin_amdgcn_s_setprio(2);
#pragma unroll
      for (int k = 0; k < 4; ++k) cz = mma_bf(u32x4{0x3f803f80u, 0x3f803f80u, 0x3f803f80u, 0x3f803f80u}, cf[k], cz);
      __builtin_amdgcn_s_setprio(0);
      stx(5);
      const u32x2 gr = {pkbf(cz[0], cz[0] - rbf(cz[0])), (u32)tag};
      asm volatile("s_mov_b32 exec_hi, 0\n\tglobal_store_dwordx2 %0, %1, %2 sc1\n\ts_mov_b32 exec_hi, -1"
                   ::"v"(lane * 8), "v"(gr), "s"(ws + WS_CS + (ft * 8 + mt) * 1024 + q * 256) : "memory");
    }
    u32 tp0, tp1; u32x2 tp2; u32x4 tp3;
    {
      const float ie = __builtin_amdgcn_rcpf(fmaxf(es[0], es[1]));
      const float f0 = es[0] * ie, f1 = es[1] * ie;
      u32 pk[4];
#pragma unroll
      for (int r = 0; r < 4; ++r) {
        float v0 = pa[r][2] * f1, v1 = pa[r][3] * f1;
        asm("" : "+v"(v0));
        v0 = fmaf(pa[r][0], f0, v0);
        asm("" : "+v"(v0));
        pk[r] = pkh(v0, fmaf(pa[r][1], f0, v1));
      }
      const bool ev = (lane & 1) == 0;
      auto lo2 = [](u32 v) { return __builtin_amdgcn_perm(v, v, 0x01000100); };
      auto hi2 = [](u32 v) { return __builtin_amdgcn_perm(v, v, 0x03020302); };
      tp0 = pk[0];
      tp1 = ev ? lo2(pk[1]) : pk[1];
      tp2 = u32x2{lo2(pk[2]), ev ? pk[2] : hi2(pk[2])};
      tp3 = u32x4{lo2(pk[3]), lo2(pk[3]), ev ? pk[3] : hi2(pk[3]), hi2(pk[3])};
      asm volatile("" : "+v"(tp0), "+v"(tp1), "+v"(tp2), "+v"(tp3));
    }
    LDS char* ka0 = sm + L_KA + chl * 4160 + 64 + tt * 4;
    LDS char* ka1 = sm + L_KA + chl * 4160 + 64 + 1024 + tt * 8;
    LDS char* ka2 = sm + L_KA + chl * 4160 + 64 + 2048 + tt * 16;
    LDS char* ka3 = sm + L_KA + chl * 4160 + (lane & 15) * 4;
    asm volatile("" : "+v"(ka0), "+v"(ka1), "+v"(ka2), "+v"(ka3));
    VMCNT(13);
    BAR();
    if (!helper) {
      dma4((const char*)wd + idx * 65536 + q * 16384 + 384, sb + L_DUMMY, a2 ? 0 : (lane & 15) * 1024);
      stw(6); stx(6);
    }
    make_vd();
    asm volatile("" : "+v"(vd[0]), "+v"(vd[1]), "+v"(vd[2]), "+v"(vd[3]));
    const u32 pollz = L_POLL + q * 512;
    LDS char* tb[2][4];
#pragma unroll
    for (int n = 0; n < 4; ++n) {
      tb[0][n] = sm + q * 4096 + (4 * l4 + qq) * 128 + ((n ^ (((l4 & 1) << 1) | (qq >> 1))) << 5) + (lane & 3) * 8;
      tb[1][n] = tb[0][n] + 4 * CE;
      asm volatile("" : "+v"(tb[0][n]), "+v"(tb[1][n]));
    }
    auto polled = [&](int n) __attribute__((always_inline)) { return __all(*(LDS u64*)(sm + pollz + min(lane, n - 1) * 16) == tag); };
    u32x4 wr[7][4];
    auto wfr = [&](int c, int n) __attribute__((always_inline)) { LDS char* p = tb[c >> 2][n] + (c & 3) * CE; return trpair(p, p + 2048); };
    auto grab = [&](int g, int c) __attribute__((always_inline)) {
#pragma unroll
      for (int n = 0; n < 4; ++n) wr[g][n] = wfr(c, n);
    };
    VMCNT(12);
    BAR();
    auto mload = [&]() __attribute__((always_inline)) { dma16_lanes<32>(ws, flag_copy() + (F_M + (mt * 4 + q) * 4) * 128, sb + L_MU + q * 512, lane * 16); };
    auto mok = [&]() __attribute__((always_inline)) { return __all(((*(volatile LDS u64*)(sm + L_MU + q * 512 + lane * 8) ^ tag) & 0xffffffff0000ffffull) == 0); };
    if (!helper) {
      mload();
      stw(7); stx(7);
    }
    VMCNT(12);
    BAR();
    if (!helper) {
#pragma unroll 1
      for (int spins = 0; spins < (1 << 18) && !mok(); ++spins) { mload(); VMCNT(0); }
      auto cload = [&]() __attribute__((always_inline)) { dma16x2_sc1(ws + WS_CS + ft * 8192 + q * 2048, sb + rs, lane * 16); };
      auto cok = [&]() __attribute__((always_inline)) {
        const u32x4 a = *(volatile LDS u32x4*)(sm + rs + lane * 16), c = *(volatile LDS u32x4*)(sm + rs + 1024 + lane * 16);
        return __all(a.y == (u32)tag && a.w == (u32)tag && c.y == (u32)tag && c.w == (u32)tag);
      };
      cload();
      wdg(0, 4); wdg(1, 5);
      VMCNT(8);
#pragma unroll 1
      for (int spins = 0; spins < (1 << 18) && !cok(); ++spins) { cload(); VMCNT(0); }
    }
    BAR();
    bool uok = false;
    auto uload = [&]() __attribute__((always_inline)) { dma16x2_sc1(ws + WS_U + ch * US + (q & 1) * 2048, sb + L_U + 512 + chl * 4608 + (q & 1) * 2048, lane * 16); };
    if (!helper) {
      dma16_lanes<16>(ws, flag_copy() + (F_U + (q & 1) * 16) * 128, sb + pollz, lane * 128);
      wdg(2, 0); wdg(3, 1);
      VMCNT(8);
      uok = polled(16);
      if (uok) uload();
      grab(0, 4); grab(1, 5);
    } else {
      wait_flags<1>(ws, (F_U + (q & 1) * 16 + lane) * 128, lane < 16, tag); uload();
      uok = true;
    }
    BAR();
    if (!uok) { wait_flags<1>(ws, (F_U + (q & 1) * 16 + lane) * 128, lane < 16, tag); uload(); }
    if (!helper) wdg(4, 2);
    *(LDS u32*)ka0 = tp0;
    *(LDS u32*)(ka0 + 512) = tp1;
    *(LDS u32x2*)ka1 = tp2;
    *(LDS u32x4*)ka2 = tp3;
    *(LDS u32*)ka3 = 0u;
    *(LDS u32*)(sm + L_U + chl * 4608 + tt * 4) = 0u;
    if (helper) { grab(0, 4); grab(1, 5); VMCNT(0); }
    else { wdg(5, 3); VMCNT(8); }
    grab(2, 0); grab(3, 1);
    LGKM0();
    BAR();
    const u32 fh = (B128 * 2 + (q >> 1)) * 4096 + (q & 1) * 128;
    const u32 fl = lane * 256;
    auto apoll = [&](int left) __attribute__((always_inline)) { if (left == APOS) dma16_lanes<14>(ws, hflag_copy() + fh, sb + pollz, fl); };
    if (!helper) { VMCNT(WTH); wdg(6, 6); }
    VMCNT(WTH); wdg(7, 4);
    VMCNT(WTH); wdg(8, 5);
    VMCNT(WTH); wdg(9, 0);
    grab(4, 2); grab(5, 3); grab(6, 6);
    VMCNT(WTH); apoll(4); wdg(10, 1);
    LGKM0();
    VMCNT(WTH); apoll(3); wdg(13, 6);
    VMCNT(WTH); apoll(2); wdg(11, 2);
    VMCNT(WTH); apoll(1); wdg(12, 3);
    VMCNT(4 * APOS < WTH ? 4 * APOS : WTH);
    if (!polled(14)) wait_flags<2>(ws, fh + fl, lane < 14, tag);
    constexpr int PF = HPF;
    u32x4 hf[56];
    const u32 hoff = WS_H + ((4 * B128 + sh) * 28 + (q & 1)) * 4096 + (q >> 1) * 2048;
    auto hload = [&](int i) __attribute__((always_inline)) {
      const int t = (i % 28) >> 1, g = (t >> 1) + (t & 1) * 7;
      hf[i] = __builtin_amdgcn_raw_buffer_load_b128(wsr, lane * 16, hoff + (i / 28) * (56 * 4096) + g * 8192 + (i & 1) * 1024, 16);
    };
#pragma unroll
    for (int i = 0; i < PF; ++i) { hload(i); SCHED(); }
    LDS char* pw[2];
#pragma unroll
    for (int n = 0; n < 2; ++n) pw[n] = sm + 5 * CE + q * 4096 + (lane & 15) * 256 + (((4 * n + l4) ^ (lane & 7)) << 4);
    f32x4 acc[2][2][4];
    const f32x4 z4 = {0.f, 0.f, 0.f, 0.f};
    auto put = [&](int m, int v) __attribute__((always_inline)) {
      *(LDS f32x4*)(pw[v & 1] + ((v >> 1) & 1) * 128 + (v >> 2) * CE + (m ? 0 : L_PA - 5 * CE)) = acc[m][v >> 2][v & 3];
    };
    u32x4 wl[4], cdone;
    static_for<0, 56>([&](auto ic) __attribute__((always_inline)) {
      constexpr int i = decltype(ic)::v, m = i / 28, j = i % 28, t = j >> 1, r = i & 1;
      constexpr int LC[7] = {4, 5, 0, 1, 2, 3, 6};
      auto mm = [&](int n) __attribute__((always_inline)) { acc[m][r][n] = mma16((t & 1) ? wl[n] : wr[t >> 1][n], hf[i], t ? acc[m][r][n] : z4); };
      mm(r ? 3 : 0);
      SCHED();
      if ((t & 1) == 0) wl[2 * r] = wfr(LC[t >> 1], 2 * r);
      SCHED();
      mm(r ? 2 : 1);
      SCHED();
      if (i + PF == 28 - PF - 1) dma16_lanes<14>(ws, hflag_copy() + fh + 131072, sb + pollz, fl);
      if (i + PF == 28) {
        if (!polled(14)) wait_flags<2>(ws, fh + 131072 + fl, lane < 14, tag);
      }
      {
        constexpr int x0 = i <= PF2AT ? 0 : (i - PF2AT > PF2 ? PF2 : i - PF2AT), x1 = i + 1 <= PF2AT ? 0 : (i + 1 - PF2AT > PF2 ? PF2 : i + 1 - PF2AT);
        if (i + PF + x0 < 56) hload(i + PF + x0);
        if (x1 > x0 && i + PF + x1 < 56) hload(i + PF + x1);
      }
      SCHED();
      mm(r ? 1 : 2);
      SCHED();
      if ((t & 1) == 0) wl[2 * r + 1] = wfr(LC[t >> 1], 2 * r + 1);
      SCHED();
      mm(r ? 0 : 3);
      SCHED();
      if (i == YGO) lds_inc(sm, L_MISC + 80, lane);
      if (i == 27) { cdone = *(volatile LDS u32x4*)(sm + L_MISC + 48); }
      if (i == 29 && __builtin_amdgcn_readfirstlane(cdone.x + cdone.y + cdone.z + cdone.w) != 4) {
#pragma unroll 1
        for (int c = 0; c < 4; ++c) wait_lds_cnt(sm, L_MISC + 48 + c * 4, 1);
      }
      if (i >= 30 && i < 38) put(0, i - 30);
      if (i == 38) { lds_inc(sm, L_MISC + 64, lane); }
      SCHED();
    });
#pragma unroll
    for (int v = 0; v < 8; ++v) put(1, v);
    LGKM0();
    BAR();
  }
}

extern "C" void kernel_launch(void* const* d_in, const int* in_sizes, int n_in, void* d_out, int out_size, void* d_ws, size_t ws_size,
                              hipStream_t stream) {
  fused_kernel<<<256, 512, 0, stream>>>((const u16*)d_in[0], (const u16*)d_in[1], (const u16*)d_in[2], (const u16*)d_in[3], (const u16*)d_in[4],
                                        (const u16*)d_in[5], (const u16*)d_in[6], (const u16*)d_in[8], (const u16*)d_in[9], (const u16*)d_in[10],
                                        (u16*)d_out, (char*)d_ws);
}
```
